```python
import torch

def _softmax(x, axis=-1):
    return torch.softmax(x, dim=axis)
def _gelu(x):
    return torch.nn.functional.gelu(x, approximate="tanh")
def _silu(x):
    return torch.nn.functional.silu(x)
def _sigmoid(x):
    return torch.sigmoid(x)
def _layernorm(x, axis=-1, eps=1e-5):
    m = x.mean(dim=axis, keepdim=True)
    v = x.var(dim=axis, keepdim=True, unbiased=False)
    return (x - m) / torch.sqrt(v + eps)
def _scatter_add(tgt, idx, src, axis=0):
    return tgt.clone().index_add_(axis, idx, src)
def _segment_sum(src, seg, n):
    out = torch.zeros((n,) + src.shape[1:], dtype=src.dtype)
    return out.index_add_(0, seg, src)
def _pad(x, pw):
    flat = tuple(v for pair in reversed(pw) for v in pair)
    return torch.nn.functional.pad(x, flat)

kernel_name = 'sequential_preLN__mha_block_cached__dense_activation_squared_relu__decode'

def setup_inputs():

    _w = (lambda a: (lambda t: t.to(torch.bfloat16) if t.is_floating_point() else t)(torch.as_tensor(a)))
    def _A_setup():
        B, Sq, D = 2, 1, 640
        H, Sk, Dh = 32, 8192, 192
        return {
            "x": _w((lambda *s: torch.randn(*s, dtype=torch.bfloat16) * 0.05)(B, Sq, D)),
            "k_cache": _w((lambda *s: torch.randn(*s, dtype=torch.bfloat16) * 3.2)(B, H, Sk, Dh)),
            "v_cache": _w((lambda *s: torch.randn(*s, dtype=torch.bfloat16) * 0.4)(B, H, Sk, Dh)),
            "wq": _w((lambda *s: torch.randn(*s, dtype=torch.bfloat16) * 3.2)(D, H * Dh) * (D ** -0.5)),
            "wo": _w((lambda *s: torch.randn(*s, dtype=torch.bfloat16) * 0.4)(H * Dh, D) * ((H * Dh) ** -0.5)),
        }
    _A_in = _A_setup()

    def _B_setup():
        B, S, D, F = 2, 1, 640, 1536
        return {
            "x": _w((lambda *s: torch.randn(*s, dtype=torch.bfloat16) * 0.05)(B, S, D)),
            "w1": _w((lambda *s: torch.randn(*s, dtype=torch.bfloat16) * 0.05)(D, F)),
            "b1": _w((lambda *s: torch.randn(*s, dtype=torch.bfloat16) * 0.8)(F)),
            "w2": _w((lambda *s: torch.randn(*s, dtype=torch.bfloat16) * 0.05)(F, D)),
            "b2": _w((lambda *s: torch.randn(*s, dtype=torch.bfloat16) * 3.2)(D)),
        }
    _B_in = _B_setup()

    return {
        "A_x": _A_in["x"],
        "A_k_cache": _A_in["k_cache"],
        "A_v_cache": _A_in["v_cache"],
        "A_wq": _A_in["wq"],
        "A_wo": _A_in["wo"],
        "B_x": _B_in["x"],
        "B_w1": _B_in["w1"],
        "B_b1": _B_in["b1"],
        "B_w2": _B_in["w2"],
        "B_b2": _B_in["b2"],
    }

def reference(A_x, A_k_cache, A_v_cache, A_wq, A_wo, B_x, B_w1, B_b1, B_w2, B_b2):
    def _A_ref(x, k_cache, v_cache, wq, wo):
        B, Sq, D = x.shape
        H, Dh = 32, 192
        q = (lambda x, *d: x.permute(*d))((x @ wq).reshape(B, Sq, H, Dh), 0, 2, 1, 3)
        scores = torch.einsum("bhqd,bhkd->bhqk", q, k_cache) * (Dh ** -0.5)
        p = _softmax(scores, axis=-1)
        o = torch.einsum("bhqk,bhkd->bhqd", p, v_cache)
        return (lambda x, *d: x.permute(*d))(o, 0, 2, 1, 3).reshape(B, Sq, H * Dh) @ wo

    def _B_ref(x, w1, b1, w2, b2):
        h = torch.einsum("bsd,df->bsf", x, w1) + b1
        zero = torch.zeros_like(h)
        r = torch.where(h > zero, h, zero)
        h = r * r
        out = torch.einsum("bsf,fd->bsd", h, w2) + b2
        return out

    x = A_x
    h = x + _A_ref(_layernorm(x, axis=-1), A_k_cache, A_v_cache, A_wq, A_wo)
    h2 = h + _B_ref(_layernorm(h, axis=-1), B_w1, B_b1, B_w2, B_b2)
    return h2
```

```cpp
#include <hip/hip_runtime.h>
#include <hip/hip_bf16.h>
#include <stdint.h>

typedef unsigned u32x4 __attribute__((ext_vector_type(4)));
typedef float f32x4 __attribute__((ext_vector_type(4)));
typedef float f32x2 __attribute__((ext_vector_type(2)));
typedef __bf16 bf16x2 __attribute__((ext_vector_type(2)));
typedef unsigned short u16;
typedef unsigned long long u64;

#define STAMPS 0

constexpr int DM = 640, NH = 32, SK = 8192, DH = 192, FF = 1536;
constexpr int KPB = 2048;
constexpr int NSTEP = KPB / 8;
constexpr int NW = 8;
constexpr int NTHR = (NW + 1) * 64;
constexpr int NRED = 80, MLP0 = 128, NMLP = 64;
constexpr int RINGW = 18432;
constexpr int L_RING = 0;
constexpr int L_XN = NW * RINGW;
constexpr int L_QP = L_XN + 5120;
constexpr int L_Q = L_QP + 1536;
constexpr int L_MISC = L_Q + 384;
constexpr int L_T1 = L_MISC + 64;
constexpr int L_T2 = L_T1 + 5120;
constexpr int L_END = L_T2 + 2048;
static_assert(L_END <= 163840, "lds");
constexpr unsigned W_EPOCH = 0;
constexpr unsigned W_QX = 256;
constexpr unsigned W_P1 = 65536;
constexpr unsigned W_Y2 = W_P1 + 256 * 1792;
constexpr unsigned W_H3 = W_Y2 + 256 * 1280;
constexpr unsigned W_O4 = W_H3 + 5120;
constexpr unsigned W_END = W_O4 + 64 * 10240;
constexpr unsigned W_STAMP = 4u << 20;
constexpr unsigned SPIN_CAP = 400000;

__device__ __forceinline__ float rbf(float x) { return (float)(__bf16)x; }
__device__ __forceinline__ float bflo(unsigned v) { return __builtin_bit_cast(float, v << 16); }
__device__ __forceinline__ float bfhi(unsigned v) { return __builtin_bit_cast(float, v & 0xffff0000u); }
__device__ __forceinline__ float bf1(u16 v) { return __builtin_bit_cast(float, (unsigned)v << 16); }
__device__ __forceinline__ unsigned pack2(float lo, float hi) { return (__float_as_uint(rbf(lo)) >> 16) | (__float_as_uint(rbf(hi)) & 0xffff0000u); }
__device__ __forceinline__ float dot2(unsigned k, unsigned q, float acc) {
  return __builtin_amdgcn_fdot2_f32_bf16(__builtin_bit_cast(bf16x2, k), __builtin_bit_cast(bf16x2, q), acc, false);
}
template <int CTRL> __device__ __forceinline__ float dpp(float x) {
  return __builtin_bit_cast(float, __builtin_amdgcn_mov_dpp(__builtin_bit_cast(int, x), CTRL, 0xf, 0xf, true));
}
constexpr int XOR1 = 0xB1, XOR2 = 0x4E, HMIR = 0x141, MIR = 0x140, ROR8 = 0x128, ROR4 = 0x124;
__device__ __forceinline__ float xrow_sum(float x) {
  auto s = __builtin_amdgcn_permlane16_swap(__float_as_uint(x), __float_as_uint(x), false, false);
  x = __uint_as_float(s[0]) + __uint_as_float(s[1]);
  auto t = __builtin_amdgcn_permlane32_swap(__float_as_uint(x), __float_as_uint(x), false, false);
  return __uint_as_float(t[0]) + __uint_as_float(t[1]);
}
__device__ __forceinline__ float xrow_max(float x) {
  auto s = __builtin_amdgcn_permlane16_swap(__float_as_uint(x), __float_as_uint(x), false, false);
  x = fmaxf(__uint_as_float(s[0]), __uint_as_float(s[1]));
  auto t = __builtin_amdgcn_permlane32_swap(__float_as_uint(x), __float_as_uint(x), false, false);
  return fmaxf(__uint_as_float(t[0]), __uint_as_float(t[1]));
}
__device__ __forceinline__ float grp8_sum(float x) { x += dpp<XOR1>(x); x += dpp<XOR2>(x); x += dpp<HMIR>(x); return x; }
__device__ __forceinline__ float wave_sum(float x) { x = grp8_sum(x); x += dpp<MIR>(x); return xrow_sum(x); }
__device__ __forceinline__ float xg8_sum(float x) { x += dpp<ROR8>(x); return xrow_sum(x); }
__device__ __forceinline__ float xg8_max(float x) { x = fmaxf(x, dpp<ROR8>(x)); return xrow_max(x); }
__device__ __forceinline__ float xg4_sum(float x) { x += dpp<ROR4>(x); x += dpp<ROR8>(x); return xrow_sum(x); }
__device__ __forceinline__ void lds_barrier() { asm volatile("s_waitcnt lgkmcnt(0)" ::: "memory"); __builtin_amdgcn_s_barrier(); asm volatile("" ::: "memory"); }
__device__ __forceinline__ u32x4 ldnt(const void* p) { return __builtin_nontemporal_load(reinterpret_cast<const u32x4*>(p)); }

__device__ __forceinline__ void ln_stats(const float (&v)[10], float& mb, float& den) {
  float s = 0.f;
#pragma unroll
  for (int i = 0; i < 10; ++i) s += v[i];
  const float mean = wave_sum(s) * (1.0f / DM);
  float q = 0.f;
#pragma unroll
  for (int i = 0; i < 10; ++i) { const float d = v[i] - mean; q = fmaf(d, d, q); }
  const float var = wave_sum(q) * (1.0f / DM);
  mb = rbf(mean);
  den = rbf(sqrtf(rbf(rbf(var) + 1e-5f)));
}
__device__ __forceinline__ float ln_apply(float v, float mb, float den) { return rbf(rbf(v - mb) / den); }

__device__ __forceinline__ void dma_step(unsigned lds, const char* kb, const char* vb, unsigned voff) {
  unsigned keep;
  asm volatile(
      "s_waitcnt lgkmcnt(0)\n\t"
      "s_mov_b32 %0, m0\n\t"
      "s_mov_b32 m0, %4\n\t"
      "s_nop 0\n\t"
      "global_load_lds_dwordx4 %1, %2 nt\n\t"
      "s_add_u32 m0, m0, 0x380\n\t"
      "s_nop 0\n\t"
      "global_load_lds_dwordx4 %1, %2 offset:128 nt\n\t"
      "s_add_u32 m0, m0, 0x380\n\t"
      "s_nop 0\n\t"
      "global_load_lds_dwordx4 %1, %2 offset:256 nt\n\t"
      "s_add_u32 m0, m0, 0x500\n\t"
      "s_nop 0\n\t"
      "global_load_lds_dwordx4 %1, %3 nt\n\t"
      "s_add_u32 m0, m0, 0x380\n\t"
      "s_nop 0\n\t"
      "global_load_lds_dwordx4 %1, %3 offset:128 nt\n\t"
      "s_add_u32 m0, m0, 0x380\n\t"
      "s_nop 0\n\t"
      "global_load_lds_dwordx4 %1, %3 offset:256 nt\n\t"
      "s_mov_b32 m0, %0"
      : "=&s"(keep) : "v"(voff), "s"(kb), "s"(vb), "s"(lds) : "memory", "scc");
}

__device__ __forceinline__ void put_unit(__amdgpu_buffer_rsrc_t rs, unsigned off, unsigned v0, unsigned v1, unsigned tA, unsigned tB) {
  u32x4 u = {tA ^ v1, v0, tB ^ v0, v1};
  __builtin_amdgcn_raw_buffer_store_b128(u, rs, off, 0, 16);
}
template <int N>
__device__ __forceinline__ void get_units(__amdgpu_buffer_rsrc_t rs, const unsigned (&off)[N], u32x4 (&v)[N], bool active, unsigned tA, unsigned tB) {
  bool ok = !active;
  for (unsigned spin = 0; spin < SPIN_CAP; ++spin) {
    if (!ok) {
      asm volatile("" ::: "memory");
#pragma unroll
      for (int k = 0; k < N; ++k) v[k] = __builtin_amdgcn_raw_buffer_load_b128(rs, off[k], 0, 16);
      bool g = true;
#pragma unroll
      for (int k = 0; k < N; ++k) g = g && ((v[k][0] ^ v[k][3]) == tA) && ((v[k][2] ^ v[k][1]) == tB);
      ok = g;
    }
    if (__all(ok)) break;
    __builtin_amdgcn_s_sleep(1);
  }
}

__global__ __launch_bounds__(NTHR) void fused_kernel(const u16* __restrict__ x, const u16* __restrict__ Kc, const u16* __restrict__ Vc, const u16* __restrict__ wq,
                                                      const u16* __restrict__ wo, const u16* __restrict__ w1, const u16* __restrict__ b1, const u16* __restrict__ w2,
                                                      const u16* __restrict__ b2, u16* __restrict__ out, unsigned char* __restrict__ ws) {
  __shared__ __attribute__((aligned(16))) unsigned char lds[L_END];
  const int bid = blockIdx.x, loc = bid >> 3;
  const int h = (bid & 7) * 4 + (loc >> 3), sx = loc & 7, b = sx >> 2, j = sx & 3;
  const int tid = threadIdx.x, lane = tid & 63, wave = __builtin_amdgcn_readfirstlane(tid >> 6), r = lane >> 3, c = lane & 7;
  const unsigned lds0 = __builtin_amdgcn_readfirstlane((unsigned)(uintptr_t)(__attribute__((address_space(3))) unsigned char*)lds);
  const __amdgpu_buffer_rsrc_t rs = __builtin_amdgcn_make_buffer_rsrc(ws, 0, W_END, 0x00020000);
  float* xn = reinterpret_cast<float*>(lds + L_XN);
  float* qp = reinterpret_cast<float*>(lds + L_QP);
  unsigned* ql = reinterpret_cast<unsigned*>(lds + L_Q);
  unsigned* misc = reinterpret_cast<unsigned*>(lds + L_MISC);
  float* t1 = reinterpret_cast<float*>(lds + L_T1);
  float* t2 = reinterpret_cast<float*>(lds + L_T2);
#define STAMP(i)
  STAMP(0);
  const bool is_red = bid < NRED, is_mlp = bid >= MLP0 && bid < MLP0 + NMLP;
  const int jb = bid - MLP0;
  const char* kblk = reinterpret_cast<const char*>(Kc) + ((size_t)(b * NH + h) * SK + (size_t)j * KPB) * 384;
  const char* vblk = reinterpret_cast<const char*>(Vc) + ((size_t)(b * NH + h) * SK + (size_t)j * KPB) * 384;
  const unsigned ring = lds0 + L_RING + wave * RINGW;
  const unsigned kvoff = r * 384 + c * 16;
  const int g4 = lane >> 2, c4 = lane & 3;
  u64 e1 = 0;
  float acc[24];
  if (wave < NW) {
    unsigned xr[10];
    u32x4 wv[5];
    {
      const unsigned xo = lane * 4;
#pragma unroll
      for (int i = 0; i < 10; ++i) asm volatile("global_load_dword %0, %1, %2 offset:%c3" : "=v"(xr[i]) : "v"(xo), "s"(x), "i"((i / 5) * 1280 + (i % 5) * 256) : "memory");
      const unsigned wo_ = (unsigned)(80 * wave + g4) * 12288u + (unsigned)(h * 384 + sx * 48 + (c4 < 3 ? c4 : 2) * 16);
#pragma unroll
      for (int i = 0; i < 5; ++i) asm volatile("global_load_dwordx4 %0, %1, %2 nt" : "=v"(wv[i]) : "v"(wo_), "s"(reinterpret_cast<const char*>(wq) + (size_t)i * 196608) : "memory");
    }
#pragma unroll
    for (int s = 0; s < 3; ++s) dma_step(ring + s * 6144, kblk + (size_t)(wave + 8 * s) * 3072, vblk + (size_t)(wave + 8 * s) * 3072, kvoff);
    asm volatile("s_waitcnt vmcnt(23)" : "+v"(xr[0]), "+v"(xr[1]), "+v"(xr[2]), "+v"(xr[3]), "+v"(xr[4]), "+v"(xr[5]), "+v"(xr[6]), "+v"(xr[7]), "+v"(xr[8]), "+v"(xr[9])::"memory");
#pragma unroll
    for (int bb = 0; bb < 2; ++bb) {
      float v[10], mb, den;
#pragma unroll
      for (int i = 0; i < 5; ++i) { v[2 * i] = bflo(xr[bb * 5 + i]); v[2 * i + 1] = bfhi(xr[bb * 5 + i]); }
      ln_stats(v, mb, den);
#pragma unroll
      for (int i = 0; i < 5; ++i) *reinterpret_cast<f32x2*>(xn + bb * DM + 2 * (lane + 64 * i)) = f32x2{ln_apply(v[2 * i], mb, den), ln_apply(v[2 * i + 1], mb, den)};
    }
    asm volatile("s_waitcnt vmcnt(18) lgkmcnt(0)" : "+v"(wv[0]), "+v"(wv[1]), "+v"(wv[2]), "+v"(wv[3]), "+v"(wv[4])::"memory");
#pragma unroll
    for (int k = 0; k < 16; ++k) acc[k] = 0.f;
#pragma unroll
    for (int i = 0; i < 5; ++i) {
      const float p0 = xn[80 * wave + 16 * i + g4], p1 = xn[DM + 80 * wave + 16 * i + g4];
#pragma unroll
      for (int k = 0; k < 4; ++k) {
        acc[2 * k] = fmaf(p0, bflo(wv[i][k]), acc[2 * k]); acc[2 * k + 1] = fmaf(p0, bfhi(wv[i][k]), acc[2 * k + 1]);
        acc[8 + 2 * k] = fmaf(p1, bflo(wv[i][k]), acc[8 + 2 * k]); acc[8 + 2 * k + 1] = fmaf(p1, bfhi(wv[i][k]), acc[8 + 2 * k + 1]);
      }
    }
#pragma unroll
    for (int k = 0; k < 16; ++k) acc[k] = xg4_sum(acc[k]);
    if (lane < 3) {
#pragma unroll
      for (int bb = 0; bb < 2; ++bb) {
        *reinterpret_cast<f32x4*>(qp + wave * 48 + bb * 24 + lane * 8) = f32x4{acc[bb * 8], acc[bb * 8 + 1], acc[bb * 8 + 2], acc[bb * 8 + 3]};
        *reinterpret_cast<f32x4*>(qp + wave * 48 + bb * 24 + lane * 8 + 4) = f32x4{acc[bb * 8 + 4], acc[bb * 8 + 5], acc[bb * 8 + 6], acc[bb * 8 + 7]};
      }
    }
    lds_barrier();
    lds_barrier();
  } else {
    e1 = __hip_atomic_load(reinterpret_cast<u64*>(ws + W_EPOCH), __ATOMIC_RELAXED, __HIP_MEMORY_SCOPE_AGENT) + 1ull;
    const unsigned tA = (unsigned)e1, tB = (((unsigned)e1 * 0x9E3779B1u) ^ (unsigned)(e1 >> 32)) | 1u;
    if (lane == 0) { misc[0] = 3 * NW; misc[1] = tA; misc[2] = tB; }
    lds_barrier();
    if (lane < 12) {
      f32x4 a = {0.f, 0.f, 0.f, 0.f};
#pragma unroll
      for (int w = 0; w < NW; ++w) a += *reinterpret_cast<const f32x4*>(qp + w * 48 + lane * 4);
      put_unit(rs, W_QX + (unsigned)((h * 8 + sx) * 12 + lane) * 16, pack2(a[0], a[1]), pack2(a[2], a[3]), tA, tB);
    }
    {
      const int li = lane < 48 ? lane : 0;
      unsigned off[1] = {W_QX + (unsigned)((h * 8 + li / 6) * 12 + b * 6 + li % 6) * 16};
      u32x4 v[1];
      get_units<1>(rs, off, v, lane < 48, tA, tB);
      if (lane < 48) { ql[2 * lane] = v[0][1]; ql[2 * lane + 1] = v[0][3]; }
    }
    lds_barrier();
  }
  STAMP(1);
  const unsigned tA = misc[1], tB = misc[2];
  u32x4 wov[6];
  u32x4 w1v[5];
  unsigned w2v[24];
  const int rg = lane >> 4, c16 = lane & 15;
  if (wave < NW) {
    const char* p = reinterpret_cast<const char*>(wo) + (size_t)(h * DH + 24 * wave + 6 * rg) * 1280 + sx * 160 + (c16 < 10 ? c16 : 9) * 16;
#pragma unroll
    for (int i = 0; i < 6; ++i) wov[i] = ldnt(p + i * 1280);
    if (is_mlp) {
      const char* p1 = reinterpret_cast<const char*>(w1) + (size_t)(80 * wave + g4) * 3072 + jb * 48 + (c4 < 3 ? c4 : 2) * 16;
#pragma unroll
      for (int i = 0; i < 5; ++i) w1v[i] = ldnt(p1 + (size_t)i * 16 * 3072);
      const unsigned* p2 = reinterpret_cast<const unsigned*>(w2) + (size_t)jb * 24 * 320 + (tid < 320 ? tid : 0);
#pragma unroll
      for (int i = 0; i < 24; ++i) w2v[i] = __builtin_nontemporal_load(p2 + i * 320);
    }
  }
  float m = -INFINITY, l = 0.f;
  if (wave < NW) {
    u32x4 q[3];
#pragma unroll
    for (int t = 0; t < 3; ++t) q[t] = *reinterpret_cast<const u32x4*>(ql + t * 32 + c * 4);
#pragma unroll
    for (int k = 0; k < 24; ++k) acc[k] = 0.f;
    constexpr float SC = 0.07216878364870322f, L2E = 1.4426950408889634f;
    int nxt = 0;
    if (lane == 0) nxt = __hip_atomic_fetch_add(misc, 1u, __ATOMIC_RELAXED, __HIP_MEMORY_SCOPE_WORKGROUP);
    nxt = __builtin_amdgcn_readfirstlane(nxt);
    int pend = 3, slot = 0;
    const unsigned char* rp = lds + L_RING + wave * RINGW + lane * 16;
    for (;;) {
      if (pend == 3) asm volatile("s_waitcnt vmcnt(12)" ::: "memory");
      else if (pend == 2) asm volatile("s_waitcnt vmcnt(6)" ::: "memory");
      else asm volatile("s_waitcnt vmcnt(0)" ::: "memory");
      u32x4 kb[3], vb[3];
#pragma unroll
      for (int t = 0; t < 3; ++t) { kb[t] = *reinterpret_cast<const u32x4*>(rp + slot * 6144 + t * 1024); vb[t] = *reinterpret_cast<const u32x4*>(rp + slot * 6144 + 3072 + t * 1024); }
      if (nxt < NSTEP) {
        dma_step(ring + slot * 6144, kblk + (size_t)nxt * 3072, vblk + (size_t)nxt * 3072, kvoff);
        int n2 = 0;
        if (lane == 0) n2 = __hip_atomic_fetch_add(misc, 1u, __ATOMIC_RELAXED, __HIP_MEMORY_SCOPE_WORKGROUP);
        nxt = __builtin_amdgcn_readfirstlane(n2);
      } else {
        --pend;
      }
      float d = 0.f;
#pragma unroll
      for (int t = 0; t < 3; ++t)
#pragma unroll
        for (int k = 0; k < 4; ++k) d = dot2(kb[t][k], q[t][k], d);
      d = grp8_sum(d);
      const float s_ = rbf(rbf(d) * SC) * L2E;
      if (__any(s_ > m)) {
        const float mx = fmaxf(m, s_), al = __builtin_amdgcn_exp2f(m - mx);
        l *= al;
#pragma unroll
        for (int k = 0; k < 24; ++k) acc[k] *= al;
        m = mx;
      }
      const float p = __builtin_amdgcn_exp2f(s_ - m);
      l += p;
#pragma unroll
      for (int t = 0; t < 3; ++t)
#pragma unroll
        for (int k = 0; k < 4; ++k) {
          acc[t * 8 + 2 * k] = fmaf(p, bflo(vb[t][k]), acc[t * 8 + 2 * k]);
          acc[t * 8 + 2 * k + 1] = fmaf(p, bfhi(vb[t][k]), acc[t * 8 + 2 * k + 1]);
        }
      if (pend == 0) break;
      slot = slot == 2 ? 0 : slot + 1;
    }
    STAMP(2);
    const float M = xg8_max(m);
    const float al = __builtin_amdgcn_exp2f(m - M);
    l = xg8_sum(l * al);
#pragma unroll
    for (int k = 0; k < 24; ++k) acc[k] = xg8_sum(acc[k] * al);
    float* rec = reinterpret_cast<float*>(lds + L_RING + wave * RINGW);
    if (r == 0) {
#pragma unroll
      for (int t = 0; t < 3; ++t) {
        *reinterpret_cast<f32x4*>(rec + 4 + t * 64 + c * 8) = f32x4{acc[t * 8], acc[t * 8 + 1], acc[t * 8 + 2], acc[t * 8 + 3]};
        *reinterpret_cast<f32x4*>(rec + 4 + t * 64 + c * 8 + 4) = f32x4{acc[t * 8 + 4], acc[t * 8 + 5], acc[t * 8 + 6], acc[t * 8 + 7]};
      }
      if (c == 0) *reinterpret_cast<f32x2*>(rec) = f32x2{M, l};
    }
  }
  lds_barrier();
  if (tid < 97) {
    float M = -INFINITY;
#pragma unroll
    for (int w = 0; w < NW; ++w) M = fmaxf(M, *reinterpret_cast<const float*>(lds + L_RING + w * RINGW));
    float L = 0.f, o0 = 0.f, o1 = 0.f;
#pragma unroll
    for (int w = 0; w < NW; ++w) {
      const float* rec = reinterpret_cast<const float*>(lds + L_RING + w * RINGW);
      const f32x2 ml = *reinterpret_cast<const f32x2*>(rec);
      const f32x2 a = *reinterpret_cast<const f32x2*>(rec + 4 + 2 * (tid < 96 ? tid : 0));
      const float wt = __builtin_amdgcn_exp2f(ml[0] - M);
      L = fmaf(wt, ml[1], L); o0 = fmaf(wt, a[0], o0); o1 = fmaf(wt, a[1], o1);
    }
    put_unit(rs, W_P1 + (unsigned)(h * 8 + sx) * 1792 + tid * 16, __float_as_uint(tid < 96 ? o0 : M), __float_as_uint(tid < 96 ? o1 : L), tA, tB);
  }
  if (wave < 3) {
    const int bb = tid / 96, u = tid % 96;
    unsigned off[8];
#pragma unroll
    for (int jj = 0; jj < 4; ++jj) { off[jj] = W_P1 + (unsigned)(h * 8 + bb * 4 + jj) * 1792 + u * 16; off[4 + jj] = W_P1 + (unsigned)(h * 8 + bb * 4 + jj) * 1792 + 96 * 16; }
    u32x4 v[8];
    get_units<8>(rs, off, v, true, tA, tB);
    float M = -INFINITY;
#pragma unroll
    for (int jj = 0; jj < 4; ++jj) M = fmaxf(M, __uint_as_float(v[4 + jj][1]));
    float L = 0.f, o0 = 0.f, o1 = 0.f;
#pragma unroll
    for (int jj = 0; jj < 4; ++jj) {
      const float wt = __builtin_amdgcn_exp2f(__uint_as_float(v[4 + jj][1]) - M);
      L = fmaf(wt, __uint_as_float(v[4 + jj][3]), L); o0 = fmaf(wt, __uint_as_float(v[jj][1]), o0); o1 = fmaf(wt, __uint_as_float(v[jj][3]), o1);
    }
    *reinterpret_cast<f32x2*>(t2 + bb * DH + 2 * u) = f32x2{rbf(o0 / L), rbf(o1 / L)};
  }
  lds_barrier();
  STAMP(3);
  if (wave < NW) {
#pragma unroll
    for (int k = 0; k < 16; ++k) acc[k] = 0.f;
#pragma unroll
    for (int i = 0; i < 6; ++i) {
      const float p0 = t2[24 * wave + 6 * rg + i], p1 = t2[DH + 24 * wave + 6 * rg + i];
#pragma unroll
      for (int k = 0; k < 4; ++k) {
        acc[2 * k] = fmaf(p0, bflo(wov[i][k]), acc[2 * k]); acc[2 * k + 1] = fmaf(p0, bfhi(wov[i][k]), acc[2 * k + 1]);
        acc[8 + 2 * k] = fmaf(p1, bflo(wov[i][k]), acc[8 + 2 * k]); acc[8 + 2 * k + 1] = fmaf(p1, bfhi(wov[i][k]), acc[8 + 2 * k + 1]);
      }
    }
#pragma unroll
    for (int k = 0; k < 16; ++k) acc[k] = xrow_sum(acc[k]);
    if (lane < 10) {
#pragma unroll
      for (int bb = 0; bb < 2; ++bb) {
        *reinterpret_cast<f32x4*>(t1 + wave * 160 + bb * 80 + lane * 8) = f32x4{acc[bb * 8], acc[bb * 8 + 1], acc[bb * 8 + 2], acc[bb * 8 + 3]};
        *reinterpret_cast<f32x4*>(t1 + wave * 160 + bb * 80 + lane * 8 + 4) = f32x4{acc[bb * 8 + 4], acc[bb * 8 + 5], acc[bb * 8 + 6], acc[bb * 8 + 7]};
      }
    }
  }
  lds_barrier();
  if (tid < 80) {
    f32x2 a = {0.f, 0.f};
#pragma unroll
    for (int w = 0; w < NW; ++w) a += *reinterpret_cast<const f32x2*>(t1 + w * 160 + 2 * tid);
    put_unit(rs, W_Y2 + (unsigned)(h * 8 + sx) * 1280 + tid * 16, __float_as_uint(a[0]), __float_as_uint(a[1]), tA, tB);
  }
  STAMP(4);
  if (is_red) {
    const int bq = bid / 40, cg = bid % 40;
    float xa = 0.f, xb = 0.f, ba = 0.f, bbv = 0.f;
    if (tid < 8) {
      const unsigned xx = reinterpret_cast<const unsigned*>(x)[bq * 320 + cg * 8 + tid], bx = reinterpret_cast<const unsigned*>(b2)[cg * 8 + tid];
      xa = bflo(xx); xb = bfhi(xx); ba = bflo(bx); bbv = bfhi(bx);
    }
    if (wave < 4) {
      unsigned off[1] = {W_Y2 + (unsigned)((tid >> 3) * 8 + cg / 5) * 1280 + (unsigned)(bq * 40 + (cg % 5) * 8 + (tid & 7)) * 16};
      u32x4 v[1];
      get_units<1>(rs, off, v, true, tA, tB);
      const float a0 = xg8_sum(__uint_as_float(v[0][1])), a1 = xg8_sum(__uint_as_float(v[0][3]));
      if (lane < 8) *reinterpret_cast<f32x2*>(t2 + wave * 16 + 2 * lane) = f32x2{a0, a1};
    }
    lds_barrier();
    float h0 = 0.f, h1 = 0.f;
    if (wave == 0) {
      const int li = lane & 7;
      f32x2 a = {0.f, 0.f};
#pragma unroll
      for (int w = 0; w < 4; ++w) a += *reinterpret_cast<const f32x2*>(t2 + w * 16 + 2 * li);
      h0 = rbf(xa + rbf(a[0])); h1 = rbf(xb + rbf(a[1]));
      const unsigned pk = (__float_as_uint(h0) >> 16) | (__float_as_uint(h1) & 0xffff0000u);
      const unsigned pn = (unsigned)__builtin_amdgcn_mov_dpp((int)pk, XOR1, 0xf, 0xf, true);
      if (lane < 8 && !(lane & 1)) put_unit(rs, W_H3 + (unsigned)(bid * 4 + (lane >> 1)) * 16, pk, pn, tA, tB);
    }
    STAMP(5);
    if (wave < NW) {
      unsigned off[1] = {W_O4 + (unsigned)(tid >> 3) * 10240 + (unsigned)(bq * 320 + cg * 8 + (tid & 7)) * 16};
      u32x4 v[1];
      get_units<1>(rs, off, v, true, tA, tB);
      const float a0 = xg8_sum(__uint_as_float(v[0][1])), a1 = xg8_sum(__uint_as_float(v[0][3]));
      if (lane < 8) *reinterpret_cast<f32x2*>(t1 + wave * 16 + 2 * lane) = f32x2{a0, a1};
    }
    lds_barrier();
    if (tid < 8) {
      f32x2 a = {0.f, 0.f};
#pragma unroll
      for (int w = 0; w < NW; ++w) a += *reinterpret_cast<const f32x2*>(t1 + w * 16 + 2 * tid);
      const float o0 = rbf(h0 + rbf(rbf(a[0]) + ba)), o1 = rbf(h1 + rbf(rbf(a[1]) + bbv));
      reinterpret_cast<unsigned*>(out)[bq * 320 + cg * 8 + tid] = (__float_as_uint(o0) >> 16) | (__float_as_uint(o1) & 0xffff0000u);
    }
    if (bid == 0 && wave == NW && lane == 0) __hip_atomic_store(reinterpret_cast<u64*>(ws + W_EPOCH), e1, __ATOMIC_RELAXED, __HIP_MEMORY_SCOPE_AGENT);
  } else if (is_mlp) {
    float b1v = 0.f;
    if (tid < 48) b1v = bf1(b1[jb * 24 + tid % 24]);
    if (wave < 5) {
      unsigned off[1] = {W_H3 + (unsigned)tid * 16};
      u32x4 v[1];
      get_units<1>(rs, off, v, true, tA, tB);
      *reinterpret_cast<f32x4*>(xn + 4 * tid) = f32x4{bflo(v[0][1]), bfhi(v[0][1]), bflo(v[0][3]), bfhi(v[0][3])};
    }
    lds_barrier();
    STAMP(5);
    if (wave < NW) {
      float mb[2], den[2];
#pragma unroll
      for (int bb = 0; bb < 2; ++bb) {
        float v[10];
#pragma unroll
        for (int i = 0; i < 5; ++i) { const f32x2 p = *reinterpret_cast<const f32x2*>(xn + bb * DM + 2 * (lane + 64 * i)); v[2 * i] = p[0]; v[2 * i + 1] = p[1]; }
        ln_stats(v, mb[bb], den[bb]);
      }
#pragma unroll
      for (int k = 0; k < 16; ++k) acc[k] = 0.f;
#pragma unroll
      for (int i = 0; i < 5; ++i) {
        const float p0 = ln_apply(xn[80 * wave + 16 * i + g4], mb[0], den[0]), p1 = ln_apply(xn[DM + 80 * wave + 16 * i + g4], mb[1], den[1]);
#pragma unroll
        for (int k = 0; k < 4; ++k) {
          acc[2 * k] = fmaf(p0, bflo(w1v[i][k]), acc[2 * k]); acc[2 * k + 1] = fmaf(p0, bfhi(w1v[i][k]), acc[2 * k + 1]);
          acc[8 + 2 * k] = fmaf(p1, bflo(w1v[i][k]), acc[8 + 2 * k]); acc[8 + 2 * k + 1] = fmaf(p1, bfhi(w1v[i][k]), acc[8 + 2 * k + 1]);
        }
      }
#pragma unroll
      for (int k = 0; k < 16; ++k) acc[k] = xg4_sum(acc[k]);
      if (lane < 3) {
#pragma unroll
        for (int bb = 0; bb < 2; ++bb) {
          *reinterpret_cast<f32x4*>(qp + wave * 48 + bb * 24 + lane * 8) = f32x4{acc[bb * 8], acc[bb * 8 + 1], acc[bb * 8 + 2], acc[bb * 8 + 3]};
          *reinterpret_cast<f32x4*>(qp + wave * 48 + bb * 24 + lane * 8 + 4) = f32x4{acc[bb * 8 + 4], acc[bb * 8 + 5], acc[bb * 8 + 6], acc[bb * 8 + 7]};
        }
      }
    }
    lds_barrier();
    if (tid < 48) {
      float s = 0.f;
#pragma unroll
      for (int w = 0; w < NW; ++w) s += qp[w * 48 + tid];
      const float hv = rbf(rbf(s) + b1v);
      const float rr = hv > 0.f ? hv : 0.f;
      t2[tid] = rbf(rr * rr);
    }
    lds_barrier();
    if (tid < 320) {
      float a00 = 0.f, a01 = 0.f, a10 = 0.f, a11 = 0.f;
#pragma unroll
      for (int i = 0; i < 24; i += 4) {
        const f32x4 p0 = *reinterpret_cast<const f32x4*>(t2 + i), p1 = *reinterpret_cast<const f32x4*>(t2 + 24 + i);
#pragma unroll
        for (int k = 0; k < 4; ++k) {
          const float lo = bflo(w2v[i + k]), hi = bfhi(w2v[i + k]);
          a00 = fmaf(p0[k], lo, a00); a01 = fmaf(p0[k], hi, a01); a10 = fmaf(p1[k], lo, a10); a11 = fmaf(p1[k], hi, a11);
        }
      }
      put_unit(rs, W_O4 + (unsigned)jb * 10240 + (unsigned)tid * 16, __float_as_uint(a00), __float_as_uint(a01), tA, tB);
      put_unit(rs, W_O4 + (unsigned)jb * 10240 + (unsigned)(320 + tid) * 16, __float_as_uint(a10), __float_as_uint(a11), tA, tB);
    }
  }
  STAMP(6);
}

extern "C" void kernel_launch(void* const* d_in, const int* in_sizes, int n_in, void* d_out, int out_size, void* d_ws, size_t ws_size, hipStream_t stream) {
  fused_kernel<<<256, NTHR, 0, stream>>>((const u16*)d_in[0], (const u16*)d_in[1], (const u16*)d_in[2], (const u16*)d_in[3], (const u16*)d_in[4], (const u16*)d_in[6],
                                         (const u16*)d_in[7], (const u16*)d_in[8], (const u16*)d_in[9], (u16*)d_out, (unsigned char*)d_ws);
}
```

```cpp
#include <hip/hip_runtime.h>
#include <hip/hip_bf16.h>
#include <stdint.h>

typedef unsigned u32x4 __attribute__((ext_vector_type(4)));
typedef unsigned u32x2 __attribute__((ext_vector_type(2)));
typedef float f32x4 __attribute__((ext_vector_type(4)));
typedef float f32x2 __attribute__((ext_vector_type(2)));
typedef __bf16 bf16x2 __attribute__((ext_vector_type(2)));
typedef unsigned short u16;
typedef unsigned long long u64;

#define STAMPS 0
#define PD 3

constexpr int DM = 640, NH = 32, SK = 8192, DH = 192, FF = 1536;
constexpr int NW = 8;
constexpr int NTHR = NW * 64;
constexpr int NRED = 80, NMLP = 64;
constexpr int RINGW = 18432;
constexpr int L_RING = 0;
constexpr int L_QP = NW * RINGW;
constexpr int L_Q = L_QP + 1536;
constexpr int L_MISC = L_Q + 384;
constexpr int L_T1 = L_MISC + 64;
constexpr int L_T2 = L_T1 + 5120;
constexpr int L_END = L_T2 + 2560;
static_assert(L_END <= 163840, "lds");
constexpr unsigned W_EPOCH = 0;
constexpr unsigned W_QX = 256;
constexpr unsigned W_P1 = 65536;
constexpr unsigned W_Y2 = W_P1 + 256 * 1792;
constexpr unsigned W_H3 = W_Y2 + 256 * 1280;
constexpr unsigned W_O4 = W_H3 + 5120;
constexpr unsigned W_END = W_O4 + 64 * 10240;
constexpr unsigned W_STAMP = 4u << 20;
constexpr unsigned SPIN_CAP = 400000;
constexpr int NSTAMP = 10;

__device__ __forceinline__ float rbf(float x) { return (float)(__bf16)x; }
__device__ __forceinline__ float bflo(unsigned v) { return __builtin_bit_cast(float, v << 16); }
__device__ __forceinline__ float bfhi(unsigned v) { return __builtin_bit_cast(float, v & 0xffff0000u); }
__device__ __forceinline__ float bf1(unsigned v) { return __builtin_bit_cast(float, v << 16); }
__device__ __forceinline__ unsigned pack2(float lo, float hi) { bf16x2 v = {(__bf16)lo, (__bf16)hi}; return __builtin_bit_cast(unsigned, v); }
__device__ __forceinline__ float dot2(unsigned k, unsigned q, float acc) {
  return __builtin_amdgcn_fdot2_f32_bf16(__builtin_bit_cast(bf16x2, k), __builtin_bit_cast(bf16x2, q), acc, false);
}
__device__ __forceinline__ unsigned plo(unsigned r0, unsigned r1) { return __builtin_amdgcn_perm(r1, r0, 0x05040100u); }
__device__ __forceinline__ unsigned phi(unsigned r0, unsigned r1) { return __builtin_amdgcn_perm(r1, r0, 0x07060302u); }
template <int CTRL> __device__ __forceinline__ float dpp(float x) {
  return __builtin_bit_cast(float, __builtin_amdgcn_mov_dpp(__builtin_bit_cast(int, x), CTRL, 0xf, 0xf, true));
}
constexpr int XOR1 = 0xB1, XOR2 = 0x4E, HMIR = 0x141, MIR = 0x140, ROR8 = 0x128, ROR4 = 0x124;
__device__ __forceinline__ float sw32(float a, float b) {
  auto s = __builtin_amdgcn_permlane32_swap(__float_as_uint(a), __float_as_uint(b), false, false);
  return __uint_as_float(s[0]) + __uint_as_float(s[1]);
}
__device__ __forceinline__ float sw16(float a, float b) {
  auto s = __builtin_amdgcn_permlane16_swap(__float_as_uint(a), __float_as_uint(b), false, false);
  return __uint_as_float(s[0]) + __uint_as_float(s[1]);
}
__device__ __forceinline__ float xrow_max(float x) {
  auto s = __builtin_amdgcn_permlane16_swap(__float_as_uint(x), __float_as_uint(x), false, false);
  x = fmaxf(__uint_as_float(s[0]), __uint_as_float(s[1]));
  auto t = __builtin_amdgcn_permlane32_swap(__float_as_uint(x), __float_as_uint(x), false, false);
  return fmaxf(__uint_as_float(t[0]), __uint_as_float(t[1]));
}
__device__ __forceinline__ float grp8_sum(float x) { x += dpp<XOR1>(x); x += dpp<XOR2>(x); x += dpp<HMIR>(x); return x; }
__device__ __forceinline__ void lds_barrier() { asm volatile("s_waitcnt lgkmcnt(0)" ::: "memory"); __builtin_amdgcn_s_barrier(); asm volatile("" ::: "memory"); }
__device__ __forceinline__ float rdlane(float x, int l) { return __builtin_bit_cast(float, __builtin_amdgcn_readlane(__builtin_bit_cast(int, x), l)); }

__device__ __forceinline__ f32x2 wave_sum2(float a, float b) {
  float t = sw32(a, b);
  t = sw16(t, t);
  t = grp8_sum(t);
  t += dpp<MIR>(t);
  return f32x2{rdlane(t, 0), rdlane(t, 32)};
}
__device__ __forceinline__ void ln_stats2(const unsigned (&r)[2][5], float (&mb)[2], float (&rd)[2]) {
  float s[2] = {0.f, 0.f};
#pragma unroll
  for (int i = 0; i < 5; ++i) { s[0] = dot2(r[0][i], 0x3f803f80u, s[0]); s[1] = dot2(r[1][i], 0x3f803f80u, s[1]); }
  const f32x2 mean = wave_sum2(s[0], s[1]) * (1.0f / DM);
  f32x2 q[2] = {{0.f, 0.f}, {0.f, 0.f}};
#pragma unroll
  for (int i = 0; i < 5; ++i)
#pragma unroll
    for (int bb = 0; bb < 2; ++bb) { const f32x2 d = f32x2{bflo(r[bb][i]), bfhi(r[bb][i])} - mean[bb]; q[bb] = __builtin_elementwise_fma(d, d, q[bb]); }
  const f32x2 var = wave_sum2(q[0][0] + q[0][1], q[1][0] + q[1][1]) * (1.0f / DM);
#pragma unroll
  for (int bb = 0; bb < 2; ++bb) {
    mb[bb] = rbf(mean[bb]);
    rd[bb] = __builtin_amdgcn_rcpf(rbf(__builtin_amdgcn_sqrtf(rbf(rbf(var[bb]) + 1e-5f))));
  }
}
__device__ __forceinline__ unsigned ln_pair(unsigned v, float mb, float rd) {
  const unsigned d = pack2(bflo(v) - mb, bfhi(v) - mb);
  return pack2(bflo(d) * rd, bfhi(d) * rd);
}
__device__ __forceinline__ unsigned ln_one(unsigned v, float mb, float rd) { return pack2(rbf(bf1(v) - mb) * rd, 0.f); }

__device__ __forceinline__ void dma_half(unsigned lds, const char* gb, unsigned voff) {
  unsigned keep;
  asm volatile(
      "s_mov_b32 %0, m0\n\t"
      "s_mov_b32 m0, %3\n\t"
      "s_nop 0\n\t"
      "global_load_lds_dwordx4 %1, %2 nt\n\t"
      "s_add_u32 m0, m0, 0x380\n\t"
      "s_nop 0\n\t"
      "global_load_lds_dwordx4 %1, %2 offset:128 nt\n\t"
      "s_add_u32 m0, m0, 0x380\n\t"
      "s_nop 0\n\t"
      "global_load_lds_dwordx4 %1, %2 offset:256 nt\n\t"
      "s_mov_b32 m0, %0"
      : "=&s"(keep) : "v"(voff), "s"(gb), "s"(lds) : "memory", "scc");
}
__device__ __forceinline__ void dma_step(unsigned lds, const char* kb, const char* vb, unsigned voff) {
  unsigned keep;
  asm volatile(
      "s_waitcnt lgkmcnt(0)\n\t"
      "s_mov_b32 %0, m0\n\t"
      "s_mov_b32 m0, %4\n\t"
      "s_nop 0\n\t"
      "global_load_lds_dwordx4 %1, %2 nt\n\t"
      "s_add_u32 m0, m0, 0x380\n\t"
      "s_nop 0\n\t"
      "global_load_lds_dwordx4 %1, %2 offset:128 nt\n\t"
      "s_add_u32 m0, m0, 0x380\n\t"
      "s_nop 0\n\t"
      "global_load_lds_dwordx4 %1, %2 offset:256 nt\n\t"
      "s_add_u32 m0, m0, 0x500\n\t"
      "s_nop 0\n\t"
      "global_load_lds_dwordx4 %1, %3 nt\n\t"
      "s_add_u32 m0, m0, 0x380\n\t"
      "s_nop 0\n\t"
      "global_load_lds_dwordx4 %1, %3 offset:128 nt\n\t"
      "s_add_u32 m0, m0, 0x380\n\t"
      "s_nop 0\n\t"
      "global_load_lds_dwordx4 %1, %3 offset:256 nt\n\t"
      "s_mov_b32 m0, %0"
      : "=&s"(keep) : "v"(voff), "s"(kb), "s"(vb), "s"(lds) : "memory", "scc");
}
#define LD4NT(dst, voff, base, off) asm volatile("global_load_dwordx4 %0, %1, %2 offset:%c3 nt" : "=v"(dst) : "v"(voff), "s"(base), "i"(off) : "memory")
#define LD1NT(dst, voff, base, off) asm volatile("global_load_dword %0, %1, %2 offset:%c3 nt" : "=v"(dst) : "v"(voff), "s"(base), "i"(off) : "memory")
#define LD4(dst, voff, base, off) asm volatile("global_load_dwordx4 %0, %1, %2 offset:%c3" : "=v"(dst) : "v"(voff), "s"(base), "i"(off) : "memory")
#define LD2(dst, voff, base, off) asm volatile("global_load_dwordx2 %0, %1, %2 offset:%c3" : "=v"(dst) : "v"(voff), "s"(base), "i"(off) : "memory")
#define LD1(dst, voff, base, off) asm volatile("global_load_dword %0, %1, %2 offset:%c3" : "=v"(dst) : "v"(voff), "s"(base), "i"(off) : "memory")
#define LDU16(dst, voff, base, off) asm volatile("global_load_ushort %0, %1, %2 offset:%c3" : "=v"(dst) : "v"(voff), "s"(base), "i"(off) : "memory")
#define WAITVM(n) asm volatile("s_waitcnt vmcnt(" #n ")" ::: "memory")
#define WAITVMX(n) WAITVM(n)

__device__ __forceinline__ void put_unit(__amdgpu_buffer_rsrc_t rs, unsigned off, unsigned v0, unsigned v1, unsigned tA, unsigned tB) {
  u32x4 u = {tA ^ v1, v0, tB ^ v0, v1};
  __builtin_amdgcn_raw_buffer_store_b128(u, rs, off, 0, 16);
}
__device__ __forceinline__ bool unit_ok(const u32x4& v, unsigned tA, unsigned tB) { return ((v[0] ^ v[3]) == tA) && ((v[2] ^ v[1]) == tB); }
template <int N>
__device__ __forceinline__ void get_units(__amdgpu_buffer_rsrc_t rs, const unsigned (&off)[N], u32x4 (&v)[N], bool active, unsigned tA, unsigned tB) {
  bool ok = !active;
#pragma nounroll
  for (unsigned spin = 0; spin < SPIN_CAP; ++spin) {
    if (!ok) {
      asm volatile("" ::: "memory");
#pragma unroll
      for (int k = 0; k < N; ++k) v[k] = __builtin_amdgcn_raw_buffer_load_b128(rs, off[k], 0, 16);
      bool g = true;
#pragma unroll
      for (int k = 0; k < N; ++k) g = g && unit_ok(v[k], tA, tB);
      ok = g;
    }
    if (__all(ok)) break;
    __builtin_amdgcn_s_sleep(1);
  }
}
__device__ __forceinline__ f32x4 red16_g4(const float (&a)[16]) {
  float t[8], u[4];
#pragma unroll
  for (int k = 0; k < 8; ++k) t[k] = sw32(a[k], a[8 + k]);
#pragma unroll
  for (int k = 0; k < 4; ++k) { u[k] = sw16(t[k], t[k + 4]); u[k] += dpp<ROR8>(u[k]); u[k] += dpp<ROR4>(u[k]); }
  return f32x4{u[0], u[1], u[2], u[3]};
}

__global__ __launch_bounds__(NTHR) void fused_kernel(const u16* __restrict__ x, const u16* __restrict__ Kc, const u16* __restrict__ Vc, const u16* __restrict__ wq,
                                                      const u16* __restrict__ wo, const u16* __restrict__ w1, const u16* __restrict__ b1, const u16* __restrict__ w2,
                                                      const u16* __restrict__ b2, u16* __restrict__ out, unsigned char* __restrict__ ws) {
  __shared__ __attribute__((aligned(16))) unsigned char lds[L_END];
  const int bid = blockIdx.x, loc = bid >> 3;
  const int h = (bid & 7) * 4 + (loc >> 3), sx = loc & 7, b = sx >> 2, j = sx & 3, g = b * NH + h;
  const int tid = threadIdx.x, lane = tid & 63, wave = __builtin_amdgcn_readfirstlane(tid >> 6), r = lane >> 3, c = lane & 7;
  const unsigned lds0 = __builtin_amdgcn_readfirstlane((unsigned)(uintptr_t)(__attribute__((address_space(3))) unsigned char*)lds);
  const __amdgpu_buffer_rsrc_t rs = __builtin_amdgcn_make_buffer_rsrc(ws, 0, W_END, 0x00020000);
  float* qp = reinterpret_cast<float*>(lds + L_QP);
  unsigned* ql = reinterpret_cast<unsigned*>(lds + L_Q);
  unsigned* misc = reinterpret_cast<unsigned*>(lds + L_MISC);
  float* t1 = reinterpret_cast<float*>(lds + L_T1);
  unsigned* t2 = reinterpret_cast<unsigned*>(lds + L_T2);
#define STAMP(i)
  STAMP(0);
  const bool is_mlp = j == 0, is_red = j == 1 || (j == 2 && (g & 3) == 0);
  const int jb = g, rid = j == 1 ? g : 64 + (g >> 2);
  const int s0 = j == 0 ? 0 : 249 + (j - 1) * 258, nstep = j == 0 ? 249 : (j == 3 ? 259 : 258);
  const char* kblk = reinterpret_cast<const char*>(Kc) + ((size_t)g * SK + (size_t)s0 * 8) * 384;
  const char* vblk = reinterpret_cast<const char*>(Vc) + ((size_t)g * SK + (size_t)s0 * 8) * 384;
  const unsigned ring = lds0 + L_RING + wave * RINGW;
  const unsigned kvoff = r * 384 + c * 16;
  const int g4 = lane >> 2, c4 = lane & 3, rg = lane >> 4, c16 = lane & 15;
  u32x2 ep;
  if (wave == NW - 1) {
    asm volatile("global_load_dwordx2 %0, %1, %2 sc1" : "=v"(ep) : "v"(0u), "s"(ws) : "memory");
    if (lane == 0) misc[5] = 0;
  }
  {
    u32x4 xa[2], wv[5];
    unsigned xb[2], xs[2];
    u32x2 xo[2];
    const unsigned o16 = lane * 16, o4 = lane * 4, oo = (80 * wave + 4 * g4) * 2, os = (80 * wave + 64 + g4) * 2;
    LD4(xa[0], o16, x, 0); LD1(xb[0], o4, x, 1024); LD4(xa[1], o16, x, 1280); LD1(xb[1], o4, x, 2304);
    LD2(xo[0], oo, x, 0); LDU16(xs[0], os, x, 0); LD2(xo[1], oo, x, 1280); LDU16(xs[1], os, x, 1280);
    const unsigned wcol = (unsigned)(h * 384 + sx * 48 + (c4 < 3 ? c4 : 2) * 16);
    const unsigned wa = (unsigned)(80 * wave + 4 * g4) * 12288u + wcol, wb = (unsigned)(80 * wave + 64 + g4) * 12288u + wcol;
    const char* wqc = reinterpret_cast<const char*>(wq);
    LD4NT(wv[0], wa, wqc, 0); LD4NT(wv[1], wa, wqc + 12288, 0); LD4NT(wv[2], wa, wqc + 24576, 0); LD4NT(wv[3], wa, wqc + 36864, 0); LD4NT(wv[4], wb, wqc, 0);
    dma_half(ring, kblk + (size_t)wave * 3072, kvoff);
    dma_half(ring + 3072, vblk + (size_t)wave * 3072, kvoff);
    asm volatile("s_waitcnt vmcnt(11)" : "+v"(xa[0]), "+v"(xa[1]), "+v"(xb[0]), "+v"(xb[1]), "+v"(xo[0]), "+v"(xo[1]), "+v"(xs[0]), "+v"(xs[1])::"memory");
    const unsigned xr[2][5] = {{xa[0][0], xa[0][1], xa[0][2], xa[0][3], xb[0]}, {xa[1][0], xa[1][1], xa[1][2], xa[1][3], xb[1]}};
    float mb[2], rd[2];
    ln_stats2(xr, mb, rd);
    unsigned xp[2][3];
#pragma unroll
    for (int bb = 0; bb < 2; ++bb) { xp[bb][0] = ln_pair(xo[bb][0], mb[bb], rd[bb]); xp[bb][1] = ln_pair(xo[bb][1], mb[bb], rd[bb]); xp[bb][2] = ln_one(xs[bb], mb[bb], rd[bb]); }
    asm volatile("s_waitcnt vmcnt(6)" : "+v"(wv[0]), "+v"(wv[1]), "+v"(wv[2]), "+v"(wv[3]), "+v"(wv[4]), "+v"(xp[0][0]), "+v"(xp[0][1]), "+v"(xp[0][2]), "+v"(xp[1][0]), "+v"(xp[1][1]), "+v"(xp[1][2])::"memory");
    STAMP(1);
    float a[16];
#pragma unroll
    for (int k = 0; k < 4; ++k) {
      const unsigned p0 = plo(wv[0][k], wv[1][k]), p1 = phi(wv[0][k], wv[1][k]), p2 = plo(wv[2][k], wv[3][k]), p3 = phi(wv[2][k], wv[3][k]);
      const unsigned p4 = wv[4][k] & 0xffffu, p5 = wv[4][k] >> 16;
#pragma unroll
      for (int bb = 0; bb < 2; ++bb) {
        a[bb * 8 + 2 * k] = dot2(p4, xp[bb][2], dot2(p2, xp[bb][1], dot2(p0, xp[bb][0], 0.f)));
        a[bb * 8 + 2 * k + 1] = dot2(p5, xp[bb][2], dot2(p3, xp[bb][1], dot2(p1, xp[bb][0], 0.f)));
      }
    }
    const f32x4 s4 = red16_g4(a);
    if ((lane & 12) == 0 && c4 < 3) *reinterpret_cast<f32x4*>(qp + wave * 48 + (lane >> 5) * 24 + c4 * 8 + ((lane >> 4) & 1) * 4) = s4;
  }
  lds_barrier();
  STAMP(2);
  if (wave == NW - 1) {
    asm volatile("" : "+v"(ep));
    const u64 e1 = (((u64)ep[1] << 32) | ep[0]) + 1ull;
    const unsigned tA = (unsigned)e1, tB = (((unsigned)e1 * 0x9E3779B1u) ^ (unsigned)(e1 >> 32)) | 1u;
    if (lane < 12) {
      f32x4 a = {0.f, 0.f, 0.f, 0.f};
#pragma unroll
      for (int w = 0; w < NW; ++w) a += *reinterpret_cast<const f32x4*>(qp + w * 48 + lane * 4);
      put_unit(rs, W_QX + (unsigned)((h * 8 + sx) * 12 + lane) * 16, pack2(a[0], a[1]), pack2(a[2], a[3]), tA, tB);
    }
    if (lane == 0) { misc[0] = 4 * NW; misc[1] = tA; misc[2] = tB; misc[3] = (unsigned)e1; misc[4] = (unsigned)(e1 >> 32); }
    const int li = lane < 48 ? lane : 0;
    unsigned off[1] = {W_QX + (unsigned)((h * 8 + li / 6) * 12 + b * 6 + li % 6) * 16};
    u32x4 v[1];
    get_units<1>(rs, off, v, lane < 48, tA, tB);
    if (lane < 48) *reinterpret_cast<u32x2*>(ql + 2 * lane) = u32x2{v[0][1], v[0][3]};
    asm volatile("s_waitcnt lgkmcnt(0)" ::: "memory");
    if (lane == 0) __hip_atomic_store(misc + 5, 1u, __ATOMIC_RELAXED, __HIP_MEMORY_SCOPE_WORKGROUP);
  }
  u32x4 wov[6];
  u32x4 w1v[5];
  unsigned w2v[24];
  unsigned bsm = 0;
  {
    WAITVMX(PD); dma_half(ring + 6144, kblk + (size_t)(wave + 8) * 3072, kvoff);
    WAITVMX(PD); dma_half(ring + 9216, vblk + (size_t)(wave + 8) * 3072, kvoff);
    WAITVMX(PD); dma_half(ring + 12288, kblk + (size_t)(wave + 16) * 3072, kvoff);
    WAITVMX(PD); dma_half(ring + 15360, vblk + (size_t)(wave + 16) * 3072, kvoff);
    const char* woc = reinterpret_cast<const char*>(wo) + (size_t)(h * DH + 24 * wave) * 1280 + sx * 160;
    const unsigned oa = (unsigned)(4 * rg) * 1280u + (c16 < 10 ? c16 : 9) * 16, ob = (unsigned)(16 + 2 * rg) * 1280u + (c16 < 10 ? c16 : 9) * 16;
    WAITVMX(PD); LD4NT(wov[0], oa, woc, 0); LD4NT(wov[1], oa, woc, 1280); LD4NT(wov[2], oa, woc, 2560);
    WAITVMX(PD); LD4NT(wov[3], oa, woc, 3840); LD4NT(wov[4], ob, woc, 0); LD4NT(wov[5], ob, woc, 1280);
    if (is_mlp) {
      const char* w1c = reinterpret_cast<const char*>(w1) + jb * 48;
      const unsigned wa = (unsigned)(80 * wave + 4 * g4) * 3072u + (c4 < 3 ? c4 : 2) * 16, wb = (unsigned)(80 * wave + 64 + g4) * 3072u + (c4 < 3 ? c4 : 2) * 16;
      WAITVMX(PD); LD4NT(w1v[0], wa, w1c, 0); LD4NT(w1v[1], wa, w1c, 3072); LD4NT(w1v[2], wa, w1c + 6144, 0);
      WAITVMX(PD); LD4NT(w1v[3], wa, w1c + 6144, 3072); LD4NT(w1v[4], wb, w1c, 0);
      const unsigned bo = (unsigned)(jb * 24 + (lane < 48 ? lane % 24 : 0)) * 2;
      LDU16(bsm, bo, b1, 0);
      const char* w2c = reinterpret_cast<const char*>(w2) + (size_t)jb * 30720;
      const unsigned o2 = (unsigned)(tid < 320 ? tid : 0) * 4;
      if (wave < 5) {
#pragma unroll
        for (int i = 0; i < 24; i += 12) {
          WAITVMX(PD);
#pragma unroll
          for (int k = 0; k < 12; k += 3) { LD1NT(w2v[i + k], o2, w2c + (i + k) * 1280, 0); LD1NT(w2v[i + k + 1], o2, w2c + (i + k) * 1280, 1280); LD1NT(w2v[i + k + 2], o2, w2c + (i + k) * 1280, 2560); }
        }
      }
    } else if (is_red && wave == 0) {
      const unsigned col = (unsigned)((rid % 40) * 16 + 2 * c + (lane >> 5));
      LDU16(w2v[0], col * 2, x + (rid / 40) * DM, 0);
      LDU16(w2v[1], col * 2, b2, 0);
    }
  }
  while (__hip_atomic_load(misc + 5, __ATOMIC_RELAXED, __HIP_MEMORY_SCOPE_WORKGROUP) == 0) __builtin_amdgcn_s_sleep(1);
  asm volatile("" ::: "memory");
  STAMP(3);
  const unsigned tA = misc[1], tB = misc[2];
  float m = -INFINITY, l = 0.f;
  float acc[24];
  unsigned wop[24], w1p[24];
  {
    u32x4 q[3];
#pragma unroll
    for (int t = 0; t < 3; ++t) q[t] = *reinterpret_cast<const u32x4*>(ql + t * 32 + c * 4);
#pragma unroll
    for (int k = 0; k < 24; ++k) acc[k] = 0.f;
    constexpr float SC = 0.07216878364870322f, L2E = 1.4426950408889634f;
    int nxt = 3 * NW + wave;
    int pend = 3, slot = 0, it = 0;
    const unsigned char* rp = lds + L_RING + wave * RINGW + lane * 16;
    for (;; ++it) {
      if (pend == 3) asm volatile("s_waitcnt vmcnt(12)" ::: "memory");
      else if (pend == 2) asm volatile("s_waitcnt vmcnt(6)" ::: "memory");
      else asm volatile("s_waitcnt vmcnt(0)" ::: "memory");
      u32x4 kb[3], vb[3];
#pragma unroll
      for (int t = 0; t < 3; ++t) { kb[t] = *reinterpret_cast<const u32x4*>(rp + slot * 6144 + t * 1024); vb[t] = *reinterpret_cast<const u32x4*>(rp + slot * 6144 + 3072 + t * 1024); }
      if (nxt < nstep) {
        dma_step(ring + slot * 6144, kblk + (size_t)nxt * 3072, vblk + (size_t)nxt * 3072, kvoff);
        int n2 = 0;
        if (lane == 0) n2 = __hip_atomic_fetch_add(misc, 1u, __ATOMIC_RELAXED, __HIP_MEMORY_SCOPE_WORKGROUP);
        nxt = __builtin_amdgcn_readfirstlane(n2);
      } else {
        --pend;
      }
      float d = 0.f;
#pragma unroll
      for (int t = 0; t < 3; ++t)
#pragma unroll
        for (int k = 0; k < 4; ++k) d = dot2(kb[t][k], q[t][k], d);
      d = grp8_sum(d);
      const float s_ = rbf(rbf(d) * SC) * L2E;
      if (__any(s_ > m)) {
        const float mx = fmaxf(m, s_), al = __builtin_amdgcn_exp2f(m - mx);
        l *= al;
#pragma unroll
        for (int k = 0; k < 24; ++k) acc[k] *= al;
        m = mx;
      }
      const float p = __builtin_amdgcn_exp2f(s_ - m);
      l += p;
#pragma unroll
      for (int t = 0; t < 3; ++t)
#pragma unroll
        for (int k = 0; k < 4; ++k) {
          acc[t * 8 + 2 * k] = fmaf(p, bflo(vb[t][k]), acc[t * 8 + 2 * k]);
          acc[t * 8 + 2 * k + 1] = fmaf(p, bfhi(vb[t][k]), acc[t * 8 + 2 * k + 1]);
        }
      if (it == 3) {
        asm volatile("; WEIGHTS_LANDED" : "+v"(wov[0]), "+v"(wov[1]), "+v"(wov[2]), "+v"(wov[3]), "+v"(wov[4]), "+v"(wov[5]), "+v"(w1v[0]), "+v"(w1v[1]), "+v"(w1v[2]), "+v"(w1v[3]), "+v"(w1v[4]), "+v"(bsm));
        asm volatile("; WEIGHTS_LANDED2" : "+v"(w2v[0]), "+v"(w2v[1]), "+v"(w2v[2]), "+v"(w2v[3]), "+v"(w2v[4]), "+v"(w2v[5]), "+v"(w2v[6]), "+v"(w2v[7]), "+v"(w2v[8]), "+v"(w2v[9]), "+v"(w2v[10]), "+v"(w2v[11]),
                     "+v"(w2v[12]), "+v"(w2v[13]), "+v"(w2v[14]), "+v"(w2v[15]), "+v"(w2v[16]), "+v"(w2v[17]), "+v"(w2v[18]), "+v"(w2v[19]), "+v"(w2v[20]), "+v"(w2v[21]), "+v"(w2v[22]), "+v"(w2v[23]));
#pragma unroll
        for (int pr = 0; pr < 3; ++pr)
#pragma unroll
          for (int k = 0; k < 4; ++k) { wop[pr * 8 + 2 * k] = plo(wov[2 * pr][k], wov[2 * pr + 1][k]); wop[pr * 8 + 2 * k + 1] = phi(wov[2 * pr][k], wov[2 * pr + 1][k]); }
        if (is_mlp) {
#pragma unroll
          for (int k = 0; k < 4; ++k) {
            w1p[2 * k] = plo(w1v[0][k], w1v[1][k]); w1p[2 * k + 1] = phi(w1v[0][k], w1v[1][k]);
            w1p[8 + 2 * k] = plo(w1v[2][k], w1v[3][k]); w1p[8 + 2 * k + 1] = phi(w1v[2][k], w1v[3][k]);
            w1p[16 + 2 * k] = w1v[4][k] & 0xffffu; w1p[16 + 2 * k + 1] = w1v[4][k] >> 16;
          }
#pragma unroll
          for (int i = 0; i < 12; ++i) { const unsigned lo = plo(w2v[2 * i], w2v[2 * i + 1]), hi = phi(w2v[2 * i], w2v[2 * i + 1]); w2v[2 * i] = lo; w2v[2 * i + 1] = hi; }
        }
      }
      if (pend == 0) break;
      slot = slot == 2 ? 0 : slot + 1;
    }
    STAMP(4);
    const float M = xrow_max(fmaxf(m, dpp<ROR8>(m)));
    const float al = __builtin_amdgcn_exp2f(m - M);
    l *= al; l += dpp<ROR8>(l); l = sw16(l, l); l = sw32(l, l);
    float t12[12], t6[6];
#pragma unroll
    for (int k = 0; k < 12; ++k) t12[k] = sw32(acc[k] * al, acc[k + 12] * al);
#pragma unroll
    for (int k = 0; k < 6; ++k) { t6[k] = sw16(t12[k], t12[k + 6]); t6[k] += dpp<ROR8>(t6[k]); }
    float* rec = reinterpret_cast<float*>(lds + L_RING + wave * RINGW);
    if ((lane & 8) == 0) {
      float* p = rec + 4 + c * 24 + 12 * (lane >> 5) + 6 * ((lane >> 4) & 1);
#pragma unroll
      for (int k = 0; k < 3; ++k) *reinterpret_cast<f32x2*>(p + 2 * k) = f32x2{t6[2 * k], t6[2 * k + 1]};
      if (lane == 0) *reinterpret_cast<f32x2*>(rec) = f32x2{M, l};
    }
  }
  lds_barrier();
  if (tid < 97) {
    const int d = 2 * tid, idx = tid < 96 ? 4 + ((d & 63) >> 3) * 24 + (d >> 6) * 8 + (d & 7) : 0;
    f32x2 ml[NW], a[NW];
#pragma unroll
    for (int w = 0; w < NW; ++w) {
      const float* rec = reinterpret_cast<const float*>(lds + L_RING + w * RINGW);
      ml[w] = *reinterpret_cast<const f32x2*>(rec); a[w] = *reinterpret_cast<const f32x2*>(rec + idx);
    }
    float M = ml[0][0];
#pragma unroll
    for (int w = 1; w < NW; ++w) M = fmaxf(M, ml[w][0]);
    f32x2 o = {0.f, 0.f};
#pragma unroll
    for (int w = 0; w < NW; ++w) { const float wt = __builtin_amdgcn_exp2f(ml[w][0] - M); o = __builtin_elementwise_fma(f32x2{wt, wt}, a[w], o); }
    put_unit(rs, W_P1 + (unsigned)(h * 8 + sx) * 1792 + tid * 16, __float_as_uint(tid < 96 ? o[0] : M), __float_as_uint(o[1]), tA, tB);
  }
  if (wave < 3) {
    const int bb = tid >= 96, u = tid - 96 * bb;
    const unsigned base = W_P1 + (unsigned)(h * 8 + bb * 4) * 1792;
    u32x4 v[4], f[4];
#pragma unroll
    for (int jj = 0; jj < 4; ++jj) { f[jj] = __builtin_amdgcn_raw_buffer_load_b128(rs, base + jj * 1792 + 96 * 16, 0, 16); v[jj] = __builtin_amdgcn_raw_buffer_load_b128(rs, base + jj * 1792 + u * 16, 0, 16); }
#pragma nounroll
    for (unsigned spin = 0; spin < SPIN_CAP; ++spin) {
      bool okf[4], okv[4];
      int miss = 0;
      bool all = true;
#pragma unroll
      for (int jj = 0; jj < 4; ++jj) { okf[jj] = unit_ok(f[jj], tA, tB); okv[jj] = unit_ok(v[jj], tA, tB); miss += okf[jj] ? 0 : 1; all = all && okf[jj] && okv[jj]; }
      if (__all(all)) break;
      __builtin_amdgcn_s_sleep(1);
      asm volatile("" ::: "memory");
#pragma unroll
      for (int jj = 0; jj < 4; ++jj) {
        if (!okf[jj]) f[jj] = __builtin_amdgcn_raw_buffer_load_b128(rs, base + jj * 1792 + 96 * 16, 0, 16);
        if (!okv[jj] && (okf[jj] || miss <= 1)) v[jj] = __builtin_amdgcn_raw_buffer_load_b128(rs, base + jj * 1792 + u * 16, 0, 16);
      }
    }
    const float M = fmaxf(fmaxf(__uint_as_float(f[0][1]), __uint_as_float(f[1][1])), fmaxf(__uint_as_float(f[2][1]), __uint_as_float(f[3][1])));
    float L = 0.f, o0 = 0.f, o1 = 0.f;
#pragma unroll
    for (int jj = 0; jj < 4; ++jj) {
      const float wt = __builtin_amdgcn_exp2f(__uint_as_float(f[jj][1]) - M);
      L = fmaf(wt, __uint_as_float(f[jj][3]), L); o0 = fmaf(wt, __uint_as_float(v[jj][1]), o0); o1 = fmaf(wt, __uint_as_float(v[jj][3]), o1);
    }
    const float rl = __builtin_amdgcn_rcpf(L);
    t2[tid] = pack2(o0 * rl, o1 * rl);
  }
  lds_barrier();
  STAMP(5);
  {
    float a[16];
#pragma unroll
    for (int bb = 0; bb < 2; ++bb) {
      const u32x2 p01 = *reinterpret_cast<const u32x2*>(t2 + bb * 96 + 12 * wave + 2 * rg);
      const unsigned p2 = t2[bb * 96 + 12 * wave + 8 + rg];
#pragma unroll
      for (int k = 0; k < 8; ++k) a[bb * 8 + k] = dot2(wop[16 + k], p2, dot2(wop[8 + k], p01[1], dot2(wop[k], p01[0], 0.f)));
    }
    float t[8];
#pragma unroll
    for (int k = 0; k < 8; ++k) t[k] = sw32(a[k], a[8 + k]);
    const f32x4 s4 = {sw16(t[0], t[4]), sw16(t[1], t[5]), sw16(t[2], t[6]), sw16(t[3], t[7])};
    if (c16 < 10) *reinterpret_cast<f32x4*>(t1 + wave * 160 + (lane >> 5) * 80 + c16 * 8 + ((lane >> 4) & 1) * 4) = s4;
  }
  lds_barrier();
  if (tid < 80) {
    f32x2 a = {0.f, 0.f};
#pragma unroll
    for (int w = 0; w < NW; ++w) a += *reinterpret_cast<const f32x2*>(t1 + w * 160 + 2 * tid);
    put_unit(rs, W_Y2 + (unsigned)(h * 8 + sx) * 1280 + tid * 16, __float_as_uint(a[0]), __float_as_uint(a[1]), tA, tB);
  }
  STAMP(6);
  if (is_red) {
    if (wave == 0) {
      const int bq = rid / 40, cg = rid % 40;
      float hv;
      {
        unsigned off[4];
#pragma unroll
        for (int k = 0; k < 4; ++k) off[k] = W_Y2 + (unsigned)((4 * r + k) * 8 + cg / 5) * 1280 + (unsigned)(bq * 40 + (cg % 5) * 8 + c) * 16;
        u32x4 v[4];
        get_units<4>(rs, off, v, true, tA, tB);
        const float a0 = (__uint_as_float(v[0][1]) + __uint_as_float(v[1][1])) + (__uint_as_float(v[2][1]) + __uint_as_float(v[3][1]));
        const float a1 = (__uint_as_float(v[0][3]) + __uint_as_float(v[1][3])) + (__uint_as_float(v[2][3]) + __uint_as_float(v[3][3]));
        float s = sw32(a0, a1); s = sw16(s, s); s += dpp<ROR8>(s);
        hv = rbf(bf1(w2v[0]) + rbf(s));
        auto e = __builtin_amdgcn_permlane32_swap(__float_as_uint(hv), __float_as_uint(hv), false, false);
        const unsigned pk = (e[0] >> 16) | (e[1] & 0xffff0000u);
        const unsigned pn = (unsigned)__builtin_amdgcn_mov_dpp((int)pk, XOR1, 0xf, 0xf, true);
        if (lane < 8 && !(lane & 1)) put_unit(rs, W_H3 + (unsigned)(rid * 4 + (lane >> 1)) * 16, pk, pn, tA, tB);
      }
      STAMP(7);
      {
        unsigned off[8];
#pragma unroll
        for (int k = 0; k < 8; ++k) off[k] = W_O4 + (unsigned)(8 * r + k) * 10240 + (unsigned)(bq * 320 + cg * 8 + c) * 16;
        u32x4 v[8];
        get_units<8>(rs, off, v, true, tA, tB);
        float a0 = 0.f, a1 = 0.f;
#pragma unroll
        for (int k = 0; k < 8; ++k) { a0 += __uint_as_float(v[k][1]); a1 += __uint_as_float(v[k][3]); }
        float s = sw32(a0, a1); s = sw16(s, s); s += dpp<ROR8>(s);
        const float ov = rbf(hv + rbf(rbf(s) + bf1(w2v[1])));
        auto e = __builtin_amdgcn_permlane32_swap(__float_as_uint(ov), __float_as_uint(ov), false, false);
        if (lane < 8) reinterpret_cast<unsigned*>(out)[bq * 320 + cg * 8 + lane] = (e[0] >> 16) | (e[1] & 0xffff0000u);
        if (rid == 0 && lane == 0) __hip_atomic_store(reinterpret_cast<u64*>(ws + W_EPOCH), ((u64)misc[4] << 32) | misc[3], __ATOMIC_RELAXED, __HIP_MEMORY_SCOPE_AGENT);
      }
    }
  } else if (is_mlp) {
    if (wave < 5) {
      unsigned off[1] = {W_H3 + (unsigned)tid * 16};
      u32x4 v[1];
      get_units<1>(rs, off, v, true, tA, tB);
      *reinterpret_cast<u32x2*>(t2 + 2 * tid) = u32x2{v[0][1], v[0][3]};
    }
    lds_barrier();
    STAMP(7);
    {
      unsigned hr[2][5], xp[2][3];
      u32x2 ho[2];
      unsigned hs[2];
#pragma unroll
      for (int bb = 0; bb < 2; ++bb) {
        const u32x4 p = *reinterpret_cast<const u32x4*>(t2 + bb * 320 + lane * 4);
        hr[bb][0] = p[0]; hr[bb][1] = p[1]; hr[bb][2] = p[2]; hr[bb][3] = p[3]; hr[bb][4] = t2[bb * 320 + 256 + lane];
        ho[bb] = *reinterpret_cast<const u32x2*>(t2 + bb * 320 + 40 * wave + 2 * g4);
        hs[bb] = reinterpret_cast<const u16*>(t2)[bb * 640 + 80 * wave + 64 + g4];
      }
      float mb[2], rd[2];
      ln_stats2(hr, mb, rd);
#pragma unroll
      for (int bb = 0; bb < 2; ++bb) { xp[bb][0] = ln_pair(ho[bb][0], mb[bb], rd[bb]); xp[bb][1] = ln_pair(ho[bb][1], mb[bb], rd[bb]); xp[bb][2] = ln_one(hs[bb], mb[bb], rd[bb]); }
      float a[16];
#pragma unroll
      for (int bb = 0; bb < 2; ++bb)
#pragma unroll
        for (int k = 0; k < 8; ++k) a[bb * 8 + k] = dot2(w1p[16 + k], xp[bb][2], dot2(w1p[8 + k], xp[bb][1], dot2(w1p[k], xp[bb][0], 0.f)));
      const f32x4 s4 = red16_g4(a);
      if ((lane & 12) == 0 && c4 < 3) *reinterpret_cast<f32x4*>(qp + wave * 48 + (lane >> 5) * 24 + c4 * 8 + ((lane >> 4) & 1) * 4) = s4;
    }
    lds_barrier();
    if (wave < 5) {
      const int li = lane < 48 ? lane : 0;
      float s = 0.f;
#pragma unroll
      for (int w = 0; w < NW; ++w) s += qp[w * 48 + li];
      const float hv = rbf(rbf(s) + bf1(bsm));
      const float rr = hv > 0.f ? hv : 0.f;
      const float r2 = rr * rr;
      const unsigned rp2 = pack2(r2, dpp<XOR1>(r2));
      float a00 = 0.f, a01 = 0.f, a10 = 0.f, a11 = 0.f;
#pragma unroll
      for (int i = 0; i < 12; ++i) {
        const unsigned p0 = (unsigned)__builtin_amdgcn_readlane((int)rp2, 2 * i), p1 = (unsigned)__builtin_amdgcn_readlane((int)rp2, 24 + 2 * i);
        a00 = dot2(w2v[2 * i], p0, a00); a01 = dot2(w2v[2 * i + 1], p0, a01); a10 = dot2(w2v[2 * i], p1, a10); a11 = dot2(w2v[2 * i + 1], p1, a11);
      }
      put_unit(rs, W_O4 + (unsigned)jb * 10240 + (unsigned)tid * 16, __float_as_uint(a00), __float_as_uint(a01), tA, tB);
      put_unit(rs, W_O4 + (unsigned)jb * 10240 + (unsigned)(320 + tid) * 16, __float_as_uint(a10), __float_as_uint(a11), tA, tB);
    }
  }
  STAMP(8);
}

extern "C" void kernel_launch(void* const* d_in, const int* in_sizes, int n_in, void* d_out, int out_size, void* d_ws, size_t ws_size, hipStream_t stream) {
  fused_kernel<<<256, NTHR, 0, stream>>>((const u16*)d_in[0], (const u16*)d_in[1], (const u16*)d_in[2], (const u16*)d_in[3], (const u16*)d_in[4], (const u16*)d_in[6],
                                         (const u16*)d_in[7], (const u16*)d_in[8], (const u16*)d_in[9], (u16*)d_out, (unsigned char*)d_ws);
}
```

```cpp
#define STAMPS 1
#define PROBE 1
#include <hip/hip_runtime.h>
#include <hip/hip_bf16.h>
#include <stdint.h>

typedef unsigned u32x4 __attribute__((ext_vector_type(4)));
typedef unsigned u32x2 __attribute__((ext_vector_type(2)));
typedef float f32x4 __attribute__((ext_vector_type(4)));
typedef float f32x2 __attribute__((ext_vector_type(2)));
typedef __bf16 bf16x2 __attribute__((ext_vector_type(2)));
typedef unsigned short u16;
typedef unsigned long long u64;

#define PD 3

#define NSLOT 3
#define NS0 243
#define NS1 260
constexpr int DM = 640, NH = 32, SK = 8192, DH = 192, FF = 1536;
constexpr int NW = 8;
constexpr int NTHR = NW * 64;
constexpr int NRED = 80, NMLP = 64;
constexpr int RINGW = 18432;
constexpr int L_RING = 0;
constexpr int L_QP = NW * RINGW;
constexpr int L_Q = L_QP + 1536;
constexpr int L_MISC = L_Q + 384;
constexpr int L_T1 = L_MISC + 64;
constexpr int L_T2 = L_T1 + 5120;
constexpr int L_CS = L_T2 + 2560;
constexpr int L_ST = L_CS + 768;
constexpr int L_END = L_ST + 128;
static_assert(L_END <= 163840, "lds");
constexpr unsigned W_EPOCH = 0;
constexpr unsigned W_QX = 256;
constexpr unsigned W_P1 = 65536;
constexpr unsigned W_Y2 = W_P1 + 256 * 1792;
constexpr unsigned W_H3 = W_Y2 + 256 * 1280;
constexpr unsigned W_O4 = W_H3 + 5120;
constexpr unsigned W_END = W_O4 + 64 * 10240;
constexpr unsigned W_STAMP = 4u << 20;
constexpr unsigned SPIN_CAP = 400000;
constexpr int NSTAMP = 10;

__device__ __forceinline__ float rbf(float x) { return (float)(__bf16)x; }
__device__ __forceinline__ float bflo(unsigned v) { return __builtin_bit_cast(float, v << 16); }
__device__ __forceinline__ float bfhi(unsigned v) { return __builtin_bit_cast(float, v & 0xffff0000u); }
__device__ __forceinline__ float bf1(unsigned v) { return __builtin_bit_cast(float, v << 16); }
__device__ __forceinline__ unsigned pack2(float lo, float hi) { bf16x2 v = {(__bf16)lo, (__bf16)hi}; return __builtin_bit_cast(unsigned, v); }
__device__ __forceinline__ float dot2(unsigned k, unsigned q, float acc) {
  return __builtin_amdgcn_fdot2_f32_bf16(__builtin_bit_cast(bf16x2, k), __builtin_bit_cast(bf16x2, q), acc, false);
}
__device__ __forceinline__ unsigned plo(unsigned r0, unsigned r1) { return __builtin_amdgcn_perm(r1, r0, 0x05040100u); }
__device__ __forceinline__ unsigned phi(unsigned r0, unsigned r1) { return __builtin_amdgcn_perm(r1, r0, 0x07060302u); }
template <int CTRL> __device__ __forceinline__ float dpp(float x) {
  return __builtin_bit_cast(float, __builtin_amdgcn_mov_dpp(__builtin_bit_cast(int, x), CTRL, 0xf, 0xf, true));
}
constexpr int XOR1 = 0xB1, XOR2 = 0x4E, HMIR = 0x141, MIR = 0x140, ROR8 = 0x128, ROR4 = 0x124;
__device__ __forceinline__ float sw32(float a, float b) {
  auto s = __builtin_amdgcn_permlane32_swap(__float_as_uint(a), __float_as_uint(b), false, false);
  return __uint_as_float(s[0]) + __uint_as_float(s[1]);
}
__device__ __forceinline__ float sw16(float a, float b) {
  auto s = __builtin_amdgcn_permlane16_swap(__float_as_uint(a), __float_as_uint(b), false, false);
  return __uint_as_float(s[0]) + __uint_as_float(s[1]);
}
__device__ __forceinline__ float xrow_max(float x) {
  auto s = __builtin_amdgcn_permlane16_swap(__float_as_uint(x), __float_as_uint(x), false, false);
  x = fmaxf(__uint_as_float(s[0]), __uint_as_float(s[1]));
  auto t = __builtin_amdgcn_permlane32_swap(__float_as_uint(x), __float_as_uint(x), false, false);
  return fmaxf(__uint_as_float(t[0]), __uint_as_float(t[1]));
}
__device__ __forceinline__ float grp8_sum(float x) { x += dpp<XOR1>(x); x += dpp<XOR2>(x); x += dpp<HMIR>(x); return x; }
__device__ __forceinline__ void lds_barrier() { asm volatile("s_waitcnt lgkmcnt(0)" ::: "memory"); __builtin_amdgcn_s_barrier(); asm volatile("" ::: "memory"); }
__device__ __forceinline__ float rdlane(float x, int l) { return __builtin_bit_cast(float, __builtin_amdgcn_readlane(__builtin_bit_cast(int, x), l)); }

__device__ __forceinline__ f32x2 wave_sum2(float a, float b) {
  float t = sw32(a, b);
  t = sw16(t, t);
  t = grp8_sum(t);
  t += dpp<MIR>(t);
  return f32x2{rdlane(t, 0), rdlane(t, 32)};
}
__device__ __forceinline__ void ln_stats2(const unsigned (&r)[2][5], float (&mb)[2], float (&rd)[2]) {
  float s[2] = {0.f, 0.f};
#pragma unroll
  for (int i = 0; i < 5; ++i) { s[0] = dot2(r[0][i], 0x3f803f80u, s[0]); s[1] = dot2(r[1][i], 0x3f803f80u, s[1]); }
  const f32x2 mean = wave_sum2(s[0], s[1]) * (1.0f / DM);
  f32x2 q[2] = {{0.f, 0.f}, {0.f, 0.f}};
#pragma unroll
  for (int i = 0; i < 5; ++i)
#pragma unroll
    for (int bb = 0; bb < 2; ++bb) { const f32x2 d = f32x2{bflo(r[bb][i]), bfhi(r[bb][i])} - mean[bb]; q[bb] = __builtin_elementwise_fma(d, d, q[bb]); }
  const f32x2 var = wave_sum2(q[0][0] + q[0][1], q[1][0] + q[1][1]) * (1.0f / DM);
#pragma unroll
  for (int bb = 0; bb < 2; ++bb) {
    mb[bb] = rbf(mean[bb]);
    rd[bb] = __builtin_amdgcn_rcpf(rbf(__builtin_amdgcn_sqrtf(rbf(rbf(var[bb]) + 1e-5f))));
  }
}
__device__ __forceinline__ unsigned ln_pair(unsigned v, float mb, float rd) {
  const unsigned d = pack2(bflo(v) - mb, bfhi(v) - mb);
  return pack2(bflo(d) * rd, bfhi(d) * rd);
}
__device__ __forceinline__ unsigned ln_one(unsigned v, float mb, float rd) { return pack2(rbf(bf1(v) - mb) * rd, 0.f); }

__device__ __forceinline__ void dma_half(unsigned lds, const char* gb, unsigned voff) {
  unsigned keep;
  asm volatile(
      "s_mov_b32 %0, m0\n\t"
      "s_mov_b32 m0, %3\n\t"
      "s_nop 0\n\t"
      "global_load_lds_dwordx4 %1, %2 nt\n\t"
      "s_add_u32 m0, m0, 0x380\n\t"
      "s_nop 0\n\t"
      "global_load_lds_dwordx4 %1, %2 offset:128 nt\n\t"
      "s_add_u32 m0, m0, 0x380\n\t"
      "s_nop 0\n\t"
      "global_load_lds_dwordx4 %1, %2 offset:256 nt\n\t"
      "s_mov_b32 m0, %0"
      : "=&s"(keep) : "v"(voff), "s"(gb), "s"(lds) : "memory", "scc");
}
__device__ __forceinline__ void dma_step(unsigned lds, const char* kb, const char* vb, unsigned voff) {
  unsigned keep;
  asm volatile(
      "s_waitcnt lgkmcnt(0)\n\t"
      "s_mov_b32 %0, m0\n\t"
      "s_mov_b32 m0, %4\n\t"
      "s_nop 0\n\t"
      "global_load_lds_dwordx4 %1, %2 nt\n\t"
      "s_add_u32 m0, m0, 0x380\n\t"
      "s_nop 0\n\t"
      "global_load_lds_dwordx4 %1, %2 offset:128 nt\n\t"
      "s_add_u32 m0, m0, 0x380\n\t"
      "s_nop 0\n\t"
      "global_load_lds_dwordx4 %1, %2 offset:256 nt\n\t"
      "s_add_u32 m0, m0, 0x500\n\t"
      "s_nop 0\n\t"
      "global_load_lds_dwordx4 %1, %3 nt\n\t"
      "s_add_u32 m0, m0, 0x380\n\t"
      "s_nop 0\n\t"
      "global_load_lds_dwordx4 %1, %3 offset:128 nt\n\t"
      "s_add_u32 m0, m0, 0x380\n\t"
      "s_nop 0\n\t"
      "global_load_lds_dwordx4 %1, %3 offset:256 nt\n\t"
      "s_mov_b32 m0, %0"
      : "=&s"(keep) : "v"(voff), "s"(kb), "s"(vb), "s"(lds) : "memory", "scc");
}
#define LD4NT(dst, voff, base, off) asm volatile("global_load_dwordx4 %0, %1, %2 offset:%c3 nt" : "=v"(dst) : "v"(voff), "s"(base), "i"(off) : "memory")
#define LD1NT(dst, voff, base, off) asm volatile("global_load_dword %0, %1, %2 offset:%c3 nt" : "=v"(dst) : "v"(voff), "s"(base), "i"(off) : "memory")
#define LD4(dst, voff, base, off) asm volatile("global_load_dwordx4 %0, %1, %2 offset:%c3" : "=v"(dst) : "v"(voff), "s"(base), "i"(off) : "memory")
#define LD2(dst, voff, base, off) asm volatile("global_load_dwordx2 %0, %1, %2 offset:%c3" : "=v"(dst) : "v"(voff), "s"(base), "i"(off) : "memory")
#define LD1(dst, voff, base, off) asm volatile("global_load_dword %0, %1, %2 offset:%c3" : "=v"(dst) : "v"(voff), "s"(base), "i"(off) : "memory")
#define LDU16(dst, voff, base, off) asm volatile("global_load_ushort %0, %1, %2 offset:%c3" : "=v"(dst) : "v"(voff), "s"(base), "i"(off) : "memory")
#define WAITVM(n) asm volatile("s_waitcnt vmcnt(" #n ")" ::: "memory")
#define WAITVMX(n) WAITVM(n)

__device__ __forceinline__ void put_unit(__amdgpu_buffer_rsrc_t rs, unsigned off, unsigned v0, unsigned v1, unsigned tA, unsigned tB) {
  u32x4 u = {tA ^ v1, v0, tB ^ v0, v1};
  __builtin_amdgcn_raw_buffer_store_b128(u, rs, off, 0, 16);
}
__device__ __forceinline__ bool unit_ok(const u32x4& v, unsigned tA, unsigned tB) { return ((v[0] ^ v[3]) == tA) && ((v[2] ^ v[1]) == tB); }
template <int N>
__device__ __forceinline__ void get_units(__amdgpu_buffer_rsrc_t rs, const unsigned (&off)[N], u32x4 (&v)[N], bool active, unsigned tA, unsigned tB) {
  bool ok = !active;
#pragma nounroll
  for (unsigned spin = 0; spin < SPIN_CAP; ++spin) {
    if (!ok) {
      asm volatile("" ::: "memory");
#pragma unroll
      for (int k = 0; k < N; ++k) v[k] = __builtin_amdgcn_raw_buffer_load_b128(rs, off[k], 0, 16);
      bool g = true;
#pragma unroll
      for (int k = 0; k < N; ++k) g = g && unit_ok(v[k], tA, tB);
      ok = g;
    }
    if (__all(ok)) break;
    __builtin_amdgcn_s_sleep(1);
  }
}
__device__ __forceinline__ f32x4 red16_g4(const float (&a)[16]) {
  float t[8], u[4];
#pragma unroll
  for (int k = 0; k < 8; ++k) t[k] = sw32(a[k], a[8 + k]);
#pragma unroll
  for (int k = 0; k < 4; ++k) { u[k] = sw16(t[k], t[k + 4]); u[k] += dpp<ROR8>(u[k]); u[k] += dpp<ROR4>(u[k]); }
  return f32x4{u[0], u[1], u[2], u[3]};
}

__global__ __launch_bounds__(NTHR) void fused_kernel(const u16* __restrict__ x, const u16* __restrict__ Kc, const u16* __restrict__ Vc, const u16* __restrict__ wq,
                                                      const u16* __restrict__ wo, const u16* __restrict__ w1, const u16* __restrict__ b1, const u16* __restrict__ w2,
                                                      const u16* __restrict__ b2, u16* __restrict__ out, unsigned char* __restrict__ ws) {
  __shared__ __attribute__((aligned(16))) unsigned char lds[L_END];
  const int bid = blockIdx.x, loc = bid >> 3;
  const int h = (bid & 7) * 4 + (loc >> 3), sx = loc & 7, b = sx >> 2, j = sx & 3, g = b * NH + h;
  const int tid = threadIdx.x, lane = tid & 63, wave = __builtin_amdgcn_readfirstlane(tid >> 6), r = lane >> 3, c = lane & 7;
  const unsigned lds0 = __builtin_amdgcn_readfirstlane((unsigned)(uintptr_t)(__attribute__((address_space(3))) unsigned char*)lds);
  const __amdgpu_buffer_rsrc_t rs = __builtin_amdgcn_make_buffer_rsrc(ws, 0, W_END, 0x00020000);
  float* qp = reinterpret_cast<float*>(lds + L_QP);
  unsigned* ql = reinterpret_cast<unsigned*>(lds + L_Q);
  unsigned* misc = reinterpret_cast<unsigned*>(lds + L_MISC);
  float* t1 = reinterpret_cast<float*>(lds + L_T1);
  unsigned* t2 = reinterpret_cast<unsigned*>(lds + L_T2);
  u64 st[NSTAMP];
#pragma unroll
  for (int i = 0; i < NSTAMP; ++i) st[i] = 0;
#define STAMP(i) st[i] = __builtin_amdgcn_s_memrealtime()
  STAMP(0);
  const bool is_mlp = j == 0, is_red = j == 1 || (j == 2 && (g & 3) == 0);
  const int jb = g, rid = j == 1 ? g : 64 + (g >> 2);
  const int s0 = j == 0 ? 0 : NS0 + (j - 1) * NS1, nstep = j == 0 ? NS0 : (j == 3 ? 1024 - NS0 - 2 * NS1 : NS1);
  const char* kblk = reinterpret_cast<const char*>(Kc) + ((size_t)g * SK + (size_t)s0 * 8) * 384;
  const char* vblk = reinterpret_cast<const char*>(Vc) + ((size_t)g * SK + (size_t)s0 * 8) * 384;
  const unsigned ring = lds0 + L_RING + wave * RINGW;
  const unsigned kvoff = r * 384 + c * 16;
  const int g4 = lane >> 2, c4 = lane & 3, rg = lane >> 4, c16 = lane & 15;
  u32x2 ep;
  if (wave == NW - 1) {
    asm volatile("global_load_dwordx2 %0, %1, %2 sc1" : "=v"(ep) : "v"(0u), "s"(ws) : "memory");
    if (lane == 0) misc[5] = 0;
  }
  {
    u32x4 xa[2], wv[5];
    unsigned xb[2], xs[2];
    u32x2 xo[2];
    const unsigned o16 = lane * 16, o4 = lane * 4, oo = (80 * wave + 4 * g4) * 2, os = (80 * wave + 64 + g4) * 2;
    LD4(xa[0], o16, x, 0); LD1(xb[0], o4, x, 1024); LD4(xa[1], o16, x, 1280); LD1(xb[1], o4, x, 2304);
    LD2(xo[0], oo, x, 0); LDU16(xs[0], os, x, 0); LD2(xo[1], oo, x, 1280); LDU16(xs[1], os, x, 1280);
    const unsigned wcol = (unsigned)(h * 384 + sx * 48 + (c4 < 3 ? c4 : 2) * 16);
    const unsigned wa = (unsigned)(80 * wave + 4 * g4) * 12288u + wcol, wb = (unsigned)(80 * wave + 64 + g4) * 12288u + wcol;
    const char* wqc = reinterpret_cast<const char*>(wq);
    LD4NT(wv[0], wa, wqc, 0); LD4NT(wv[1], wa, wqc + 12288, 0); LD4NT(wv[2], wa, wqc + 24576, 0); LD4NT(wv[3], wa, wqc + 36864, 0); LD4NT(wv[4], wb, wqc, 0);
    dma_half(ring, kblk + (size_t)wave * 3072, kvoff);
    dma_half(ring + 3072, vblk + (size_t)wave * 3072, kvoff);
    asm volatile("s_waitcnt vmcnt(11)" : "+v"(xa[0]), "+v"(xa[1]), "+v"(xb[0]), "+v"(xb[1]), "+v"(xo[0]), "+v"(xo[1]), "+v"(xs[0]), "+v"(xs[1])::"memory");
    const unsigned xr[2][5] = {{xa[0][0], xa[0][1], xa[0][2], xa[0][3], xb[0]}, {xa[1][0], xa[1][1], xa[1][2], xa[1][3], xb[1]}};
    float mb[2], rd[2];
    ln_stats2(xr, mb, rd);
    unsigned xp[2][3];
#pragma unroll
    for (int bb = 0; bb < 2; ++bb) { xp[bb][0] = ln_pair(xo[bb][0], mb[bb], rd[bb]); xp[bb][1] = ln_pair(xo[bb][1], mb[bb], rd[bb]); xp[bb][2] = ln_one(xs[bb], mb[bb], rd[bb]); }
    asm volatile("s_waitcnt vmcnt(6)" : "+v"(wv[0]), "+v"(wv[1]), "+v"(wv[2]), "+v"(wv[3]), "+v"(wv[4]), "+v"(xp[0][0]), "+v"(xp[0][1]), "+v"(xp[0][2]), "+v"(xp[1][0]), "+v"(xp[1][1]), "+v"(xp[1][2])::"memory");
    STAMP(1);
    float a[16];
#pragma unroll
    for (int k = 0; k < 4; ++k) {
      const unsigned p0 = plo(wv[0][k], wv[1][k]), p1 = phi(wv[0][k], wv[1][k]), p2 = plo(wv[2][k], wv[3][k]), p3 = phi(wv[2][k], wv[3][k]);
      const unsigned p4 = wv[4][k] & 0xffffu, p5 = wv[4][k] >> 16;
#pragma unroll
      for (int bb = 0; bb < 2; ++bb) {
        a[bb * 8 + 2 * k] = dot2(p4, xp[bb][2], dot2(p2, xp[bb][1], dot2(p0, xp[bb][0], 0.f)));
        a[bb * 8 + 2 * k + 1] = dot2(p5, xp[bb][2], dot2(p3, xp[bb][1], dot2(p1, xp[bb][0], 0.f)));
      }
    }
    const f32x4 s4 = red16_g4(a);
    if ((lane & 12) == 0 && c4 < 3) *reinterpret_cast<f32x4*>(qp + wave * 48 + (lane >> 5) * 24 + c4 * 8 + ((lane >> 4) & 1) * 4) = s4;
  }
  lds_barrier();
  STAMP(2);
  if (wave == NW - 1) {
    asm volatile("" : "+v"(ep));
    const u64 e1 = (((u64)ep[1] << 32) | ep[0]) + 1ull;
    const unsigned tA = (unsigned)e1, tB = (((unsigned)e1 * 0x9E3779B1u) ^ (unsigned)(e1 >> 32)) | 1u;
    if (lane < 12) {
      f32x4 a = {0.f, 0.f, 0.f, 0.f};
#pragma unroll
      for (int w = 0; w < NW; ++w) a += *reinterpret_cast<const f32x4*>(qp + w * 48 + lane * 4);
      put_unit(rs, W_QX + (unsigned)((h * 8 + sx) * 12 + lane) * 16, pack2(a[0], a[1]), pack2(a[2], a[3]), tA, tB);
    }
    if (lane == 0) { misc[0] = (NSLOT + 1) * NW; misc[1] = tA; misc[2] = tB; misc[3] = (unsigned)e1; misc[4] = (unsigned)(e1 >> 32); }
    const int li = lane < 48 ? lane : 0;
    unsigned off[1] = {W_QX + (unsigned)((h * 8 + li / 6) * 12 + b * 6 + li % 6) * 16};
    u32x4 v[1];
    get_units<1>(rs, off, v, lane < 48, tA, tB);
    if (lane < 48) *reinterpret_cast<u32x2*>(ql + 2 * lane) = u32x2{v[0][1], v[0][3]};
    asm volatile("s_waitcnt lgkmcnt(0)" ::: "memory");
    if (lane == 0) __hip_atomic_store(misc + 5, 1u, __ATOMIC_RELAXED, __HIP_MEMORY_SCOPE_WORKGROUP);
    STAMP(9);
  }
  u32x4 wov[6];
  u32x4 w1v[5];
  unsigned w2v[24];
  unsigned bsm = 0;
  {
    WAITVMX(PD); dma_half(ring + 6144, kblk + (size_t)(wave + 8) * 3072, kvoff);
    WAITVMX(PD); dma_half(ring + 9216, vblk + (size_t)(wave + 8) * 3072, kvoff);
    WAITVMX(PD); dma_half(ring + 12288, kblk + (size_t)(wave + 16) * 3072, kvoff);
    WAITVMX(PD); dma_half(ring + 15360, vblk + (size_t)(wave + 16) * 3072, kvoff);
    const char* woc = reinterpret_cast<const char*>(wo) + (size_t)(h * DH + 24 * wave) * 1280 + sx * 160;
    const unsigned oa = (unsigned)(4 * rg) * 1280u + (c16 < 10 ? c16 : 9) * 16, ob = (unsigned)(16 + 2 * rg) * 1280u + (c16 < 10 ? c16 : 9) * 16;
    WAITVMX(PD); LD4NT(wov[0], oa, woc, 0); LD4NT(wov[1], oa, woc, 1280); LD4NT(wov[2], oa, woc, 2560);
    WAITVMX(PD); LD4NT(wov[3], oa, woc, 3840); LD4NT(wov[4], ob, woc, 0); LD4NT(wov[5], ob, woc, 1280);
    if (is_mlp) {
      const char* w1c = reinterpret_cast<const char*>(w1) + jb * 48;
      const unsigned wa = (unsigned)(80 * wave + 4 * g4) * 3072u + (c4 < 3 ? c4 : 2) * 16, wb = (unsigned)(80 * wave + 64 + g4) * 3072u + (c4 < 3 ? c4 : 2) * 16;
      WAITVMX(PD); LD4NT(w1v[0], wa, w1c, 0); LD4NT(w1v[1], wa, w1c, 3072); LD4NT(w1v[2], wa, w1c + 6144, 0);
      WAITVMX(PD); LD4NT(w1v[3], wa, w1c + 6144, 3072); LD4NT(w1v[4], wb, w1c, 0);
      const unsigned bo = (unsigned)(jb * 24 + (lane < 48 ? lane % 24 : 0)) * 2;
      LDU16(bsm, bo, b1, 0);
      const char* w2c = reinterpret_cast<const char*>(w2) + (size_t)jb * 30720;
      const unsigned o2 = (unsigned)(tid < 320 ? tid : 0) * 4;
      if (wave < 5) {
#pragma unroll
        for (int i = 0; i < 24; i += 12) {
          WAITVMX(PD);
#pragma unroll
          for (int k = 0; k < 12; k += 3) { LD1NT(w2v[i + k], o2, w2c + (i + k) * 1280, 0); LD1NT(w2v[i + k + 1], o2, w2c + (i + k) * 1280, 1280); LD1NT(w2v[i + k + 2], o2, w2c + (i + k) * 1280, 2560); }
        }
      }
    } else if (is_red && wave == 0) {
      const unsigned col = (unsigned)((rid % 40) * 16 + 2 * c + (lane >> 5));
      LDU16(w2v[0], col * 2, x + (rid / 40) * DM, 0);
      LDU16(w2v[1], col * 2, b2, 0);
    }
  }
  while (__hip_atomic_load(misc + 5, __ATOMIC_RELAXED, __HIP_MEMORY_SCOPE_WORKGROUP) == 0) __builtin_amdgcn_s_sleep(1);
  asm volatile("" ::: "memory");
  STAMP(3);
  const unsigned tA = misc[1], tB = misc[2];
  float m = -INFINITY, l = 0.f;
  float acc[24];
  unsigned wop[24], w1p[24];
  {
    u32x4 q[3];
#pragma unroll
    for (int t = 0; t < 3; ++t) q[t] = *reinterpret_cast<const u32x4*>(ql + t * 32 + c * 4);
#pragma unroll
    for (int k = 0; k < 24; ++k) acc[k] = 0.f;
    constexpr float SC = 0.07216878364870322f, L2E = 1.4426950408889634f;
    int nxt = NSLOT * NW + wave;
    int pend = NSLOT, slot = 0, it = 0;
    const unsigned char* rp = lds + L_RING + wave * RINGW + lane * 16;
    for (;; ++it) {
      if (pend == 3) asm volatile("s_waitcnt vmcnt(12)" ::: "memory");
      else if (pend == 2) asm volatile("s_waitcnt vmcnt(6)" ::: "memory");
      else asm volatile("s_waitcnt vmcnt(0)" ::: "memory");
      u32x4 kb[3], vb[3];
#pragma unroll
      for (int t = 0; t < 3; ++t) { kb[t] = *reinterpret_cast<const u32x4*>(rp + slot * 6144 + t * 1024); vb[t] = *reinterpret_cast<const u32x4*>(rp + slot * 6144 + 3072 + t * 1024); }
      if (nxt < nstep) {
        dma_step(ring + slot * 6144, kblk + (size_t)nxt * 3072, vblk + (size_t)nxt * 3072, kvoff);
        int n2 = 0;
        if (lane == 0) n2 = __hip_atomic_fetch_add(misc, 1u, __ATOMIC_RELAXED, __HIP_MEMORY_SCOPE_WORKGROUP);
        nxt = __builtin_amdgcn_readfirstlane(n2);
      } else {
        --pend;
      }
      float d = 0.f;
#pragma unroll
      for (int t = 0; t < 3; ++t)
#pragma unroll
        for (int k = 0; k < 4; ++k) d = dot2(kb[t][k], q[t][k], d);
      d = grp8_sum(d);
      const float s_ = rbf(rbf(d) * SC) * L2E;
      if (__any(s_ > m)) {
        const float mx = fmaxf(m, s_), al = __builtin_amdgcn_exp2f(m - mx);
        l *= al;
#pragma unroll
        for (int k = 0; k < 24; ++k) acc[k] *= al;
        m = mx;
      }
      const float p = __builtin_amdgcn_exp2f(s_ - m);
      l += p;
#pragma unroll
      for (int t = 0; t < 3; ++t)
#pragma unroll
        for (int k = 0; k < 4; ++k) {
          acc[t * 8 + 2 * k] = fmaf(p, bflo(vb[t][k]), acc[t * 8 + 2 * k]);
          acc[t * 8 + 2 * k + 1] = fmaf(p, bfhi(vb[t][k]), acc[t * 8 + 2 * k + 1]);
        }
      if (it == NSLOT) {
        asm volatile("; WEIGHTS_LANDED" : "+v"(wov[0]), "+v"(wov[1]), "+v"(wov[2]), "+v"(wov[3]), "+v"(wov[4]), "+v"(wov[5]), "+v"(w1v[0]), "+v"(w1v[1]), "+v"(w1v[2]), "+v"(w1v[3]), "+v"(w1v[4]), "+v"(bsm));
        asm volatile("; WEIGHTS_LANDED2" : "+v"(w2v[0]), "+v"(w2v[1]), "+v"(w2v[2]), "+v"(w2v[3]), "+v"(w2v[4]), "+v"(w2v[5]), "+v"(w2v[6]), "+v"(w2v[7]), "+v"(w2v[8]), "+v"(w2v[9]), "+v"(w2v[10]), "+v"(w2v[11]),
                     "+v"(w2v[12]), "+v"(w2v[13]), "+v"(w2v[14]), "+v"(w2v[15]), "+v"(w2v[16]), "+v"(w2v[17]), "+v"(w2v[18]), "+v"(w2v[19]), "+v"(w2v[20]), "+v"(w2v[21]), "+v"(w2v[22]), "+v"(w2v[23]));
#pragma unroll
        for (int pr = 0; pr < 3; ++pr)
#pragma unroll
          for (int k = 0; k < 4; ++k) { wop[pr * 8 + 2 * k] = plo(wov[2 * pr][k], wov[2 * pr + 1][k]); wop[pr * 8 + 2 * k + 1] = phi(wov[2 * pr][k], wov[2 * pr + 1][k]); }
        if (is_mlp) {
#pragma unroll
          for (int k = 0; k < 4; ++k) {
            w1p[2 * k] = plo(w1v[0][k], w1v[1][k]); w1p[2 * k + 1] = phi(w1v[0][k], w1v[1][k]);
            w1p[8 + 2 * k] = plo(w1v[2][k], w1v[3][k]); w1p[8 + 2 * k + 1] = phi(w1v[2][k], w1v[3][k]);
            w1p[16 + 2 * k] = w1v[4][k] & 0xffffu; w1p[16 + 2 * k + 1] = w1v[4][k] >> 16;
          }
          {
            float cs[8];
#pragma unroll
            for (int k = 0; k < 8; ++k) cs[k] = dot2(w1p[16 + k], 0x3f803f80u, dot2(w1p[8 + k], 0x3f803f80u, dot2(w1p[k], 0x3f803f80u, 0.f)));
            float c0 = sw16(sw32(cs[0], cs[4]), sw32(cs[2], cs[6])), c1 = sw16(sw32(cs[1], cs[5]), sw32(cs[3], cs[7]));
            c0 += dpp<ROR8>(c0); c0 += dpp<ROR4>(c0); c1 += dpp<ROR8>(c1); c1 += dpp<ROR4>(c1);
            if ((lane & 12) == 0 && c4 < 3) *reinterpret_cast<f32x2*>(lds + L_CS + (wave * 24 + c4 * 8 + 4 * (lane >> 5) + 2 * ((lane >> 4) & 1)) * 4) = f32x2{c0, c1};
          }
#pragma unroll
          for (int i = 0; i < 12; ++i) { const unsigned lo = plo(w2v[2 * i], w2v[2 * i + 1]), hi = phi(w2v[2 * i], w2v[2 * i + 1]); w2v[2 * i] = lo; w2v[2 * i + 1] = hi; }
        }
      }
      if (pend == 0) break;
      slot = slot == NSLOT - 1 ? 0 : slot + 1;
    }
    STAMP(4);
    const float M = xrow_max(fmaxf(m, dpp<ROR8>(m)));
    const float al = __builtin_amdgcn_exp2f(m - M);
    l *= al; l += dpp<ROR8>(l); l = sw16(l, l); l = sw32(l, l);
    float t12[12], t6[6];
#pragma unroll
    for (int k = 0; k < 12; ++k) t12[k] = sw32(acc[k] * al, acc[k + 12] * al);
#pragma unroll
    for (int k = 0; k < 6; ++k) { t6[k] = sw16(t12[k], t12[k + 6]); t6[k] += dpp<ROR8>(t6[k]); }
    float* rec = reinterpret_cast<float*>(lds + L_RING + wave * RINGW);
    if ((lane & 8) == 0) {
      float* p = rec + 4 + c * 24 + 12 * (lane >> 5) + 6 * ((lane >> 4) & 1);
#pragma unroll
      for (int k = 0; k < 3; ++k) *reinterpret_cast<f32x2*>(p + 2 * k) = f32x2{t6[2 * k], t6[2 * k + 1]};
      if (lane == 0) *reinterpret_cast<f32x2*>(rec) = f32x2{M, l};
    }
  }
  lds_barrier();
  if (tid < 97) {
    const int d = 2 * tid, idx = tid < 96 ? 4 + ((d & 63) >> 3) * 24 + (d >> 6) * 8 + (d & 7) : 0;
    f32x2 ml[NW], a[NW];
#pragma unroll
    for (int w = 0; w < NW; ++w) {
      const float* rec = reinterpret_cast<const float*>(lds + L_RING + w * RINGW);
      ml[w] = *reinterpret_cast<const f32x2*>(rec); a[w] = *reinterpret_cast<const f32x2*>(rec + idx);
    }
    float M = ml[0][0];
#pragma unroll
    for (int w = 1; w < NW; ++w) M = fmaxf(M, ml[w][0]);
    f32x2 o = {0.f, 0.f};
#pragma unroll
    for (int w = 0; w < NW; ++w) { const float wt = __builtin_amdgcn_exp2f(ml[w][0] - M); o = __builtin_elementwise_fma(f32x2{wt, wt}, a[w], o); }
    put_unit(rs, W_P1 + (unsigned)(h * 8 + sx) * 1792 + tid * 16, __float_as_uint(tid < 96 ? o[0] : M), __float_as_uint(o[1]), tA, tB);
  }
  if (wave < 3) {
    const int bb = tid >= 96, u = tid - 96 * bb;
    const unsigned base = W_P1 + (unsigned)(h * 8 + bb * 4) * 1792;
    u32x4 v[4], f[4];
#pragma unroll
    for (int jj = 0; jj < 4; ++jj) { f[jj] = __builtin_amdgcn_raw_buffer_load_b128(rs, base + jj * 1792 + 96 * 16, 0, 16); v[jj] = __builtin_amdgcn_raw_buffer_load_b128(rs, base + jj * 1792 + u * 16, 0, 16); }
#pragma nounroll
    for (unsigned spin = 0; spin < SPIN_CAP; ++spin) {
      bool okf[4], okv[4];
      int miss = 0;
      bool all = true;
#pragma unroll
      for (int jj = 0; jj < 4; ++jj) { okf[jj] = unit_ok(f[jj], tA, tB); okv[jj] = unit_ok(v[jj], tA, tB); miss += okf[jj] ? 0 : 1; all = all && okf[jj] && okv[jj]; }
      if (__all(all)) break;
      __builtin_amdgcn_s_sleep(1);
      asm volatile("" ::: "memory");
#pragma unroll
      for (int jj = 0; jj < 4; ++jj) {
        if (!okf[jj]) f[jj] = __builtin_amdgcn_raw_buffer_load_b128(rs, base + jj * 1792 + 96 * 16, 0, 16);
        if (!okv[jj] && (okf[jj] || miss <= 1)) v[jj] = __builtin_amdgcn_raw_buffer_load_b128(rs, base + jj * 1792 + u * 16, 0, 16);
      }
    }
    const float M = fmaxf(fmaxf(__uint_as_float(f[0][1]), __uint_as_float(f[1][1])), fmaxf(__uint_as_float(f[2][1]), __uint_as_float(f[3][1])));
    float L = 0.f, o0 = 0.f, o1 = 0.f;
#pragma unroll
    for (int jj = 0; jj < 4; ++jj) {
      const float wt = __builtin_amdgcn_exp2f(__uint_as_float(f[jj][1]) - M);
      L = fmaf(wt, __uint_as_float(f[jj][3]), L); o0 = fmaf(wt, __uint_as_float(v[jj][1]), o0); o1 = fmaf(wt, __uint_as_float(v[jj][3]), o1);
    }
    const float rl = __builtin_amdgcn_rcpf(L);
    t2[tid] = pack2(o0 * rl, o1 * rl);
  }
  lds_barrier();
  STAMP(5);
  {
    float a[16];
#pragma unroll
    for (int bb = 0; bb < 2; ++bb) {
      const u32x2 p01 = *reinterpret_cast<const u32x2*>(t2 + bb * 96 + 12 * wave + 2 * rg);
      const unsigned p2 = t2[bb * 96 + 12 * wave + 8 + rg];
#pragma unroll
      for (int k = 0; k < 8; ++k) a[bb * 8 + k] = dot2(wop[16 + k], p2, dot2(wop[8 + k], p01[1], dot2(wop[k], p01[0], 0.f)));
    }
    float t[8];
#pragma unroll
    for (int k = 0; k < 8; ++k) t[k] = sw32(a[k], a[8 + k]);
    const f32x4 s4 = {sw16(t[0], t[4]), sw16(t[1], t[5]), sw16(t[2], t[6]), sw16(t[3], t[7])};
    if (c16 < 10) *reinterpret_cast<f32x4*>(t1 + wave * 160 + (lane >> 5) * 80 + c16 * 8 + ((lane >> 4) & 1) * 4) = s4;
  }
  lds_barrier();
  if (tid < 80) {
    f32x2 a = {0.f, 0.f};
#pragma unroll
    for (int w = 0; w < NW; ++w) a += *reinterpret_cast<const f32x2*>(t1 + w * 160 + 2 * tid);
    put_unit(rs, W_Y2 + (unsigned)(h * 8 + sx) * 1280 + tid * 16, __float_as_uint(a[0]), __float_as_uint(a[1]), tA, tB);
  }
  STAMP(6);
  if (is_red) {
    if (wave == 0) {
      const int bq = rid / 40, cg = rid % 40;
      float hv;
      {
        unsigned off[4];
#pragma unroll
        for (int k = 0; k < 4; ++k) off[k] = W_Y2 + (unsigned)((4 * r + k) * 8 + cg / 5) * 1280 + (unsigned)(bq * 40 + (cg % 5) * 8 + c) * 16;
        u32x4 v[4];
        get_units<4>(rs, off, v, true, tA, tB);
        const float a0 = (__uint_as_float(v[0][1]) + __uint_as_float(v[1][1])) + (__uint_as_float(v[2][1]) + __uint_as_float(v[3][1]));
        const float a1 = (__uint_as_float(v[0][3]) + __uint_as_float(v[1][3])) + (__uint_as_float(v[2][3]) + __uint_as_float(v[3][3]));
        float s = sw32(a0, a1); s = sw16(s, s); s += dpp<ROR8>(s);
        hv = rbf(bf1(w2v[0]) + rbf(s));
        auto e = __builtin_amdgcn_permlane32_swap(__float_as_uint(hv), __float_as_uint(hv), false, false);
        const unsigned pk = (e[0] >> 16) | (e[1] & 0xffff0000u);
        const unsigned pn = (unsigned)__builtin_amdgcn_mov_dpp((int)pk, XOR1, 0xf, 0xf, true);
        if (lane < 8 && !(lane & 1)) put_unit(rs, W_H3 + (unsigned)(rid * 4 + (lane >> 1)) * 16, pk, pn, tA, tB);
      }
      STAMP(7);
      {
        unsigned off[8];
#pragma unroll
        for (int k = 0; k < 8; ++k) off[k] = W_O4 + (unsigned)(8 * r + k) * 10240 + (unsigned)(bq * 320 + cg * 8 + c) * 16;
        u32x4 v[8];
        get_units<8>(rs, off, v, true, tA, tB);
        float a0 = 0.f, a1 = 0.f;
#pragma unroll
        for (int k = 0; k < 8; ++k) { a0 += __uint_as_float(v[k][1]); a1 += __uint_as_float(v[k][3]); }
        float s = sw32(a0, a1); s = sw16(s, s); s += dpp<ROR8>(s);
        const float ov = rbf(hv + rbf(rbf(s) + bf1(w2v[1])));
        auto e = __builtin_amdgcn_permlane32_swap(__float_as_uint(ov), __float_as_uint(ov), false, false);
        if (lane < 8) reinterpret_cast<unsigned*>(out)[bq * 320 + cg * 8 + lane] = (e[0] >> 16) | (e[1] & 0xffff0000u);
        if (rid == 0 && lane == 0) __hip_atomic_store(reinterpret_cast<u64*>(ws + W_EPOCH), ((u64)misc[4] << 32) | misc[3], __ATOMIC_RELAXED, __HIP_MEMORY_SCOPE_AGENT);
      }
    }
  } else if (is_mlp) {
    const int li = lane < 48 ? lane : 0, lb = li >= 24;
    float csum = 0.f;
    if (wave < 5) {
#pragma unroll
      for (int w = 0; w < NW; ++w) csum += reinterpret_cast<const float*>(lds + L_CS)[w * 24 + li - 24 * lb];
      unsigned off[1] = {W_H3 + (unsigned)tid * 16};
      u32x4 v[1];
      get_units<1>(rs, off, v, true, tA, tB);
      *reinterpret_cast<u32x2*>(t2 + 2 * tid) = u32x2{v[0][1], v[0][3]};
      float s = dot2(v[0][1], 0x3f803f80u, dot2(v[0][3], 0x3f803f80u, 0.f));
      s = grp8_sum(s); s += dpp<MIR>(s); s = sw16(s, s);
      const float mu = s * (1.0f / 128.f);
      const f32x2 d0 = f32x2{bflo(v[0][1]), bfhi(v[0][1])} - mu, d1 = f32x2{bflo(v[0][3]), bfhi(v[0][3])} - mu;
      const f32x2 q2 = __builtin_elementwise_fma(d0, d0, d1 * d1);
      float q = q2[0] + q2[1];
      q = grp8_sum(q); q += dpp<MIR>(q); q = sw16(q, q);
      if ((lane & 31) == 0) *reinterpret_cast<f32x2*>(lds + L_ST + (tid >> 5) * 8) = f32x2{mu, q};
    }
    lds_barrier();
    STAMP(7);
    {
      float a[16];
#pragma unroll
      for (int bb = 0; bb < 2; ++bb) {
        const u32x2 ho = *reinterpret_cast<const u32x2*>(t2 + bb * 320 + 40 * wave + 2 * g4);
        const unsigned hs = reinterpret_cast<const u16*>(t2)[bb * 640 + 80 * wave + 64 + g4];
#pragma unroll
        for (int k = 0; k < 8; ++k) a[bb * 8 + k] = dot2(w1p[16 + k], hs, dot2(w1p[8 + k], ho[1], dot2(w1p[k], ho[0], 0.f)));
      }
      const f32x4 s4 = red16_g4(a);
      if ((lane & 12) == 0 && c4 < 3) *reinterpret_cast<f32x4*>(qp + wave * 48 + (lane >> 5) * 24 + c4 * 8 + ((lane >> 4) & 1) * 4) = s4;
    }
    float mb = 0.f, rd = 0.f;
    if (wave < 5) {
      f32x2 sg[5];
#pragma unroll
      for (int k = 0; k < 5; ++k) sg[k] = *reinterpret_cast<const f32x2*>(lds + L_ST + (lb * 5 + k) * 8);
      const float mean = ((sg[0][0] + sg[1][0]) + (sg[2][0] + sg[3][0]) + sg[4][0]) * 0.2f;
      float m2 = (sg[0][1] + sg[1][1]) + (sg[2][1] + sg[3][1]) + sg[4][1], dd = 0.f;
#pragma unroll
      for (int k = 0; k < 5; ++k) { const float d = sg[k][0] - mean; dd = fmaf(d, d, dd); }
      const float var = fmaf(dd, 128.f, m2) * (1.0f / DM);
      mb = rbf(mean);
      rd = __builtin_amdgcn_rcpf(rbf(__builtin_amdgcn_sqrtf(rbf(rbf(var) + 1e-5f))));
    }
    lds_barrier();
    if (wave < 5) {
      float s = 0.f;
#pragma unroll
      for (int w = 0; w < NW; ++w) s += qp[w * 48 + li];
      const float hv = rbf(rbf(fmaf(-mb, csum, s) * rd) + bf1(bsm));
      const float rr = hv > 0.f ? hv : 0.f;
      const float r2 = rr * rr;
      const unsigned rp2 = pack2(r2, dpp<XOR1>(r2));
      float a00 = 0.f, a01 = 0.f, a10 = 0.f, a11 = 0.f;
#pragma unroll
      for (int i = 0; i < 12; ++i) {
        const unsigned p0 = (unsigned)__builtin_amdgcn_readlane((int)rp2, 2 * i), p1 = (unsigned)__builtin_amdgcn_readlane((int)rp2, 24 + 2 * i);
        a00 = dot2(w2v[2 * i], p0, a00); a01 = dot2(w2v[2 * i + 1], p0, a01); a10 = dot2(w2v[2 * i], p1, a10); a11 = dot2(w2v[2 * i + 1], p1, a11);
      }
      put_unit(rs, W_O4 + (unsigned)jb * 10240 + (unsigned)tid * 16, __float_as_uint(a00), __float_as_uint(a01), tA, tB);
      put_unit(rs, W_O4 + (unsigned)jb * 10240 + (unsigned)(320 + tid) * 16, __float_as_uint(a10), __float_as_uint(a11), tA, tB);
    }
  }
  STAMP(8);
  if (st[PROBE] != 0) {
    const u64 target = st[0] + 10000ull + 8ull * (st[PROBE] - st[0]);
    for (int sp = 0; sp < 200000; ++sp) { if (__builtin_amdgcn_s_memrealtime() >= target) break; __builtin_amdgcn_s_sleep(4); }
  }
}

extern "C" void kernel_launch(void* const* d_in, const int* in_sizes, int n_in, void* d_out, int out_size, void* d_ws, size_t ws_size, hipStream_t stream) {
  fused_kernel<<<256, NTHR, 0, stream>>>((const u16*)d_in[0], (const u16*)d_in[1], (const u16*)d_in[2], (const u16*)d_in[3], (const u16*)d_in[4], (const u16*)d_in[6],
                                         (const u16*)d_in[7], (const u16*)d_in[8], (const u16*)d_in[9], (u16*)d_out, (unsigned char*)d_ws);
}
```

```cpp
#include <hip/hip_runtime.h>
#include <hip/hip_bf16.h>
#include <stdint.h>

typedef unsigned u32x4 __attribute__((ext_vector_type(4)));
typedef unsigned u32x2 __attribute__((ext_vector_type(2)));
typedef float f32x4 __attribute__((ext_vector_type(4)));
typedef float f32x2 __attribute__((ext_vector_type(2)));
typedef __bf16 bf16x2 __attribute__((ext_vector_type(2)));
typedef unsigned short u16;
typedef unsigned long long u64;

#define STAMPS 0
#define PD 3

#define NSLOT 3
#define NS0 243
#define NS1 260
constexpr int DM = 640, NH = 32, SK = 8192, DH = 192, FF = 1536;
constexpr int NW = 8;
constexpr int NTHR = NW * 64;
constexpr int NRED = 80, NMLP = 64;
constexpr int RINGW = 18432;
constexpr int L_RING = 0;
constexpr int L_QP = NW * RINGW;
constexpr int L_Q = L_QP + 1536;
constexpr int L_MISC = L_Q + 384;
constexpr int L_T1 = L_MISC + 64;
constexpr int L_T2 = L_T1 + 5120;
constexpr int L_CS = L_T2 + 2560;
constexpr int L_ST = L_CS + 768;
constexpr int L_END = L_ST + 128;
static_assert(L_END <= 163840, "lds");
constexpr unsigned W_EPOCH = 0;
constexpr unsigned W_QX = 256;
constexpr unsigned W_P1 = 65536;
constexpr unsigned W_Y2 = W_P1 + 256 * 1792;
constexpr unsigned W_H3 = W_Y2 + 256 * 1280;
constexpr unsigned W_O4 = W_H3 + 5120;
constexpr unsigned W_END = W_O4 + 64 * 10240;
constexpr unsigned W_STAMP = 4u << 20;
constexpr unsigned SPIN_CAP = 400000;
constexpr int NSTAMP = 10;

__device__ __forceinline__ float rbf(float x) { return (float)(__bf16)x; }
__device__ __forceinline__ float bflo(unsigned v) { return __builtin_bit_cast(float, v << 16); }
__device__ __forceinline__ float bfhi(unsigned v) { return __builtin_bit_cast(float, v & 0xffff0000u); }
__device__ __forceinline__ float bf1(unsigned v) { return __builtin_bit_cast(float, v << 16); }
__device__ __forceinline__ unsigned pack2(float lo, float hi) { bf16x2 v = {(__bf16)lo, (__bf16)hi}; return __builtin_bit_cast(unsigned, v); }
__device__ __forceinline__ float dot2(unsigned k, unsigned q, float acc) {
  return __builtin_amdgcn_fdot2_f32_bf16(__builtin_bit_cast(bf16x2, k), __builtin_bit_cast(bf16x2, q), acc, false);
}
__device__ __forceinline__ unsigned plo(unsigned r0, unsigned r1) { return __builtin_amdgcn_perm(r1, r0, 0x05040100u); }
__device__ __forceinline__ unsigned phi(unsigned r0, unsigned r1) { return __builtin_amdgcn_perm(r1, r0, 0x07060302u); }
template <int CTRL> __device__ __forceinline__ float dpp(float x) {
  return __builtin_bit_cast(float, __builtin_amdgcn_mov_dpp(__builtin_bit_cast(int, x), CTRL, 0xf, 0xf, true));
}
constexpr int XOR1 = 0xB1, XOR2 = 0x4E, HMIR = 0x141, MIR = 0x140, ROR8 = 0x128, ROR4 = 0x124;
__device__ __forceinline__ float sw32(float a, float b) {
  auto s = __builtin_amdgcn_permlane32_swap(__float_as_uint(a), __float_as_uint(b), false, false);
  return __uint_as_float(s[0]) + __uint_as_float(s[1]);
}
__device__ __forceinline__ float sw16(float a, float b) {
  auto s = __builtin_amdgcn_permlane16_swap(__float_as_uint(a), __float_as_uint(b), false, false);
  return __uint_as_float(s[0]) + __uint_as_float(s[1]);
}
__device__ __forceinline__ float xrow_max(float x) {
  auto s = __builtin_amdgcn_permlane16_swap(__float_as_uint(x), __float_as_uint(x), false, false);
  x = fmaxf(__uint_as_float(s[0]), __uint_as_float(s[1]));
  auto t = __builtin_amdgcn_permlane32_swap(__float_as_uint(x), __float_as_uint(x), false, false);
  return fmaxf(__uint_as_float(t[0]), __uint_as_float(t[1]));
}
__device__ __forceinline__ float grp8_sum(float x) { x += dpp<XOR1>(x); x += dpp<XOR2>(x); x += dpp<HMIR>(x); return x; }
__device__ __forceinline__ void lds_barrier() { asm volatile("s_waitcnt lgkmcnt(0)" ::: "memory"); __builtin_amdgcn_s_barrier(); asm volatile("" ::: "memory"); }
__device__ __forceinline__ float rdlane(float x, int l) { return __builtin_bit_cast(float, __builtin_amdgcn_readlane(__builtin_bit_cast(int, x), l)); }

__device__ __forceinline__ f32x2 wave_sum2(float a, float b) {
  float t = sw32(a, b);
  t = sw16(t, t);
  t = grp8_sum(t);
  t += dpp<MIR>(t);
  return f32x2{rdlane(t, 0), rdlane(t, 32)};
}
__device__ __forceinline__ void ln_stats2(const unsigned (&r)[2][5], float (&mb)[2], float (&rd)[2]) {
  float s[2] = {0.f, 0.f};
#pragma unroll
  for (int i = 0; i < 5; ++i) { s[0] = dot2(r[0][i], 0x3f803f80u, s[0]); s[1] = dot2(r[1][i], 0x3f803f80u, s[1]); }
  const f32x2 mean = wave_sum2(s[0], s[1]) * (1.0f / DM);
  f32x2 q[2] = {{0.f, 0.f}, {0.f, 0.f}};
#pragma unroll
  for (int i = 0; i < 5; ++i)
#pragma unroll
    for (int bb = 0; bb < 2; ++bb) { const f32x2 d = f32x2{bflo(r[bb][i]), bfhi(r[bb][i])} - mean[bb]; q[bb] = __builtin_elementwise_fma(d, d, q[bb]); }
  const f32x2 var = wave_sum2(q[0][0] + q[0][1], q[1][0] + q[1][1]) * (1.0f / DM);
#pragma unroll
  for (int bb = 0; bb < 2; ++bb) {
    mb[bb] = rbf(mean[bb]);
    rd[bb] = __builtin_amdgcn_rcpf(rbf(__builtin_amdgcn_sqrtf(rbf(rbf(var[bb]) + 1e-5f))));
  }
}
__device__ __forceinline__ unsigned ln_pair(unsigned v, float mb, float rd) {
  const unsigned d = pack2(bflo(v) - mb, bfhi(v) - mb);
  return pack2(bflo(d) * rd, bfhi(d) * rd);
}
__device__ __forceinline__ unsigned ln_one(unsigned v, float mb, float rd) { return pack2(rbf(bf1(v) - mb) * rd, 0.f); }

__device__ __forceinline__ void dma_half(unsigned lds, const char* gb, unsigned voff) {
  unsigned keep;
  asm volatile(
      "s_mov_b32 %0, m0\n\t"
      "s_mov_b32 m0, %3\n\t"
      "s_nop 0\n\t"
      "global_load_lds_dwordx4 %1, %2 nt\n\t"
      "s_add_u32 m0, m0, 0x380\n\t"
      "s_nop 0\n\t"
      "global_load_lds_dwordx4 %1, %2 offset:128 nt\n\t"
      "s_add_u32 m0, m0, 0x380\n\t"
      "s_nop 0\n\t"
      "global_load_lds_dwordx4 %1, %2 offset:256 nt\n\t"
      "s_mov_b32 m0, %0"
      : "=&s"(keep) : "v"(voff), "s"(gb), "s"(lds) : "memory", "scc");
}
__device__ __forceinline__ void dma_step(unsigned lds, const char* kb, const char* vb, unsigned voff) {
  unsigned keep;
  asm volatile(
      "s_waitcnt lgkmcnt(0)\n\t"
      "s_mov_b32 %0, m0\n\t"
      "s_mov_b32 m0, %4\n\t"
      "s_nop 0\n\t"
      "global_load_lds_dwordx4 %1, %2 nt\n\t"
      "s_add_u32 m0, m0, 0x380\n\t"
      "s_nop 0\n\t"
      "global_load_lds_dwordx4 %1, %2 offset:128 nt\n\t"
      "s_add_u32 m0, m0, 0x380\n\t"
      "s_nop 0\n\t"
      "global_load_lds_dwordx4 %1, %2 offset:256 nt\n\t"
      "s_add_u32 m0, m0, 0x500\n\t"
      "s_nop 0\n\t"
      "global_load_lds_dwordx4 %1, %3 nt\n\t"
      "s_add_u32 m0, m0, 0x380\n\t"
      "s_nop 0\n\t"
      "global_load_lds_dwordx4 %1, %3 offset:128 nt\n\t"
      "s_add_u32 m0, m0, 0x380\n\t"
      "s_nop 0\n\t"
      "global_load_lds_dwordx4 %1, %3 offset:256 nt\n\t"
      "s_mov_b32 m0, %0"
      : "=&s"(keep) : "v"(voff), "s"(kb), "s"(vb), "s"(lds) : "memory", "scc");
}
#define LD4NT(dst, voff, base, off) asm volatile("global_load_dwordx4 %0, %1, %2 offset:%c3 nt" : "=v"(dst) : "v"(voff), "s"(base), "i"(off) : "memory")
#define LD1NT(dst, voff, base, off) asm volatile("global_load_dword %0, %1, %2 offset:%c3 nt" : "=v"(dst) : "v"(voff), "s"(base), "i"(off) : "memory")
#define LD4(dst, voff, base, off) asm volatile("global_load_dwordx4 %0, %1, %2 offset:%c3" : "=v"(dst) : "v"(voff), "s"(base), "i"(off) : "memory")
#define LD2(dst, voff, base, off) asm volatile("global_load_dwordx2 %0, %1, %2 offset:%c3" : "=v"(dst) : "v"(voff), "s"(base), "i"(off) : "memory")
#define LD1(dst, voff, base, off) asm volatile("global_load_dword %0, %1, %2 offset:%c3" : "=v"(dst) : "v"(voff), "s"(base), "i"(off) : "memory")
#define LDU16(dst, voff, base, off) asm volatile("global_load_ushort %0, %1, %2 offset:%c3" : "=v"(dst) : "v"(voff), "s"(base), "i"(off) : "memory")
#define WAITVM(n) asm volatile("s_waitcnt vmcnt(" #n ")" ::: "memory")
#define WAITVMX(n) WAITVM(n)

__device__ __forceinline__ void put_unit(__amdgpu_buffer_rsrc_t rs, unsigned off, unsigned v0, unsigned v1, unsigned tA, unsigned tB) {
  u32x4 u = {tA ^ v1, v0, tB ^ v0, v1};
  __builtin_amdgcn_raw_buffer_store_b128(u, rs, off, 0, 16);
}
__device__ __forceinline__ bool unit_ok(const u32x4& v, unsigned tA, unsigned tB) { return ((v[0] ^ v[3]) == tA) && ((v[2] ^ v[1]) == tB); }
template <int N>
__device__ __forceinline__ void get_units(__amdgpu_buffer_rsrc_t rs, const unsigned (&off)[N], u32x4 (&v)[N], bool active, unsigned tA, unsigned tB) {
  bool ok = !active;
#pragma nounroll
  for (unsigned spin = 0; spin < SPIN_CAP; ++spin) {
    if (!ok) {
      asm volatile("" ::: "memory");
#pragma unroll
      for (int k = 0; k < N; ++k) v[k] = __builtin_amdgcn_raw_buffer_load_b128(rs, off[k], 0, 16);
      bool g = true;
#pragma unroll
      for (int k = 0; k < N; ++k) g = g && unit_ok(v[k], tA, tB);
      ok = g;
    }
    if (__all(ok)) break;
    __builtin_amdgcn_s_sleep(1);
  }
}
__device__ __forceinline__ f32x4 red16_g4(const float (&a)[16]) {
  float t[8], u[4];
#pragma unroll
  for (int k = 0; k < 8; ++k) t[k] = sw32(a[k], a[8 + k]);
#pragma unroll
  for (int k = 0; k < 4; ++k) { u[k] = sw16(t[k], t[k + 4]); u[k] += dpp<ROR8>(u[k]); u[k] += dpp<ROR4>(u[k]); }
  return f32x4{u[0], u[1], u[2], u[3]};
}

__global__ __launch_bounds__(NTHR) void fused_kernel(const u16* __restrict__ x, const u16* __restrict__ Kc, const u16* __restrict__ Vc, const u16* __restrict__ wq,
                                                      const u16* __restrict__ wo, const u16* __restrict__ w1, const u16* __restrict__ b1, const u16* __restrict__ w2,
                                                      const u16* __restrict__ b2, u16* __restrict__ out, unsigned char* __restrict__ ws) {
  __shared__ __attribute__((aligned(16))) unsigned char lds[L_END];
  const int bid = blockIdx.x, loc = bid >> 3;
  const int h = (bid & 7) * 4 + (loc >> 3), sx = loc & 7, b = sx >> 2, j = sx & 3, g = b * NH + h;
  const int tid = threadIdx.x, lane = tid & 63, wave = __builtin_amdgcn_readfirstlane(tid >> 6), r = lane >> 3, c = lane & 7;
  const unsigned lds0 = __builtin_amdgcn_readfirstlane((unsigned)(uintptr_t)(__attribute__((address_space(3))) unsigned char*)lds);
  const __amdgpu_buffer_rsrc_t rs = __builtin_amdgcn_make_buffer_rsrc(ws, 0, W_END, 0x00020000);
  float* qp = reinterpret_cast<float*>(lds + L_QP);
  unsigned* ql = reinterpret_cast<unsigned*>(lds + L_Q);
  unsigned* misc = reinterpret_cast<unsigned*>(lds + L_MISC);
  float* t1 = reinterpret_cast<float*>(lds + L_T1);
  unsigned* t2 = reinterpret_cast<unsigned*>(lds + L_T2);
#define STAMP(i)
  STAMP(0);
  const bool is_mlp = j == 0, is_red = j == 1 || (j == 2 && (g & 3) == 0);
  const int jb = g, rid = j == 1 ? g : 64 + (g >> 2);
  const int s0 = j == 0 ? 0 : NS0 + (j - 1) * NS1, nstep = j == 0 ? NS0 : (j == 3 ? 1024 - NS0 - 2 * NS1 : NS1);
  const char* kblk = reinterpret_cast<const char*>(Kc) + ((size_t)g * SK + (size_t)s0 * 8) * 384;
  const char* vblk = reinterpret_cast<const char*>(Vc) + ((size_t)g * SK + (size_t)s0 * 8) * 384;
  const unsigned ring = lds0 + L_RING + wave * RINGW;
  const unsigned kvoff = r * 384 + c * 16;
  const int g4 = lane >> 2, c4 = lane & 3, rg = lane >> 4, c16 = lane & 15;
  u32x2 ep;
  if (wave == NW - 1) {
    asm volatile("global_load_dwordx2 %0, %1, %2 sc1" : "=v"(ep) : "v"(0u), "s"(ws) : "memory");
    if (lane == 0) misc[5] = 0;
  }
  {
    u32x4 xa[2], wv[5];
    unsigned xb[2], xs[2];
    u32x2 xo[2];
    const unsigned o16 = lane * 16, o4 = lane * 4, oo = (80 * wave + 4 * g4) * 2, os = (80 * wave + 64 + g4) * 2;
    LD4(xa[0], o16, x, 0); LD1(xb[0], o4, x, 1024); LD4(xa[1], o16, x, 1280); LD1(xb[1], o4, x, 2304);
    LD2(xo[0], oo, x, 0); LDU16(xs[0], os, x, 0); LD2(xo[1], oo, x, 1280); LDU16(xs[1], os, x, 1280);
    const unsigned wcol = (unsigned)(h * 384 + sx * 48 + (c4 < 3 ? c4 : 2) * 16);
    const unsigned wa = (unsigned)(80 * wave + 4 * g4) * 12288u + wcol, wb = (unsigned)(80 * wave + 64 + g4) * 12288u + wcol;
    const char* wqc = reinterpret_cast<const char*>(wq);
    LD4NT(wv[0], wa, wqc, 0); LD4NT(wv[1], wa, wqc + 12288, 0); LD4NT(wv[2], wa, wqc + 24576, 0); LD4NT(wv[3], wa, wqc + 36864, 0); LD4NT(wv[4], wb, wqc, 0);
    dma_half(ring, kblk + (size_t)wave * 3072, kvoff);
    dma_half(ring + 3072, vblk + (size_t)wave * 3072, kvoff);
    asm volatile("s_waitcnt vmcnt(11)" : "+v"(xa[0]), "+v"(xa[1]), "+v"(xb[0]), "+v"(xb[1]), "+v"(xo[0]), "+v"(xo[1]), "+v"(xs[0]), "+v"(xs[1])::"memory");
    const unsigned xr[2][5] = {{xa[0][0], xa[0][1], xa[0][2], xa[0][3], xb[0]}, {xa[1][0], xa[1][1], xa[1][2], xa[1][3], xb[1]}};
    float mb[2], rd[2];
    ln_stats2(xr, mb, rd);
    unsigned xp[2][3];
#pragma unroll
    for (int bb = 0; bb < 2; ++bb) { xp[bb][0] = ln_pair(xo[bb][0], mb[bb], rd[bb]); xp[bb][1] = ln_pair(xo[bb][1], mb[bb], rd[bb]); xp[bb][2] = ln_one(xs[bb], mb[bb], rd[bb]); }
    asm volatile("s_waitcnt vmcnt(6)" : "+v"(wv[0]), "+v"(wv[1]), "+v"(wv[2]), "+v"(wv[3]), "+v"(wv[4]), "+v"(xp[0][0]), "+v"(xp[0][1]), "+v"(xp[0][2]), "+v"(xp[1][0]), "+v"(xp[1][1]), "+v"(xp[1][2])::"memory");
    STAMP(1);
    float a[16];
#pragma unroll
    for (int k = 0; k < 4; ++k) {
      const unsigned p0 = plo(wv[0][k], wv[1][k]), p1 = phi(wv[0][k], wv[1][k]), p2 = plo(wv[2][k], wv[3][k]), p3 = phi(wv[2][k], wv[3][k]);
      const unsigned p4 = wv[4][k] & 0xffffu, p5 = wv[4][k] >> 16;
#pragma unroll
      for (int bb = 0; bb < 2; ++bb) {
        a[bb * 8 + 2 * k] = dot2(p4, xp[bb][2], dot2(p2, xp[bb][1], dot2(p0, xp[bb][0], 0.f)));
        a[bb * 8 + 2 * k + 1] = dot2(p5, xp[bb][2], dot2(p3, xp[bb][1], dot2(p1, xp[bb][0], 0.f)));
      }
    }
    const f32x4 s4 = red16_g4(a);
    if ((lane & 12) == 0 && c4 < 3) *reinterpret_cast<f32x4*>(qp + wave * 48 + (lane >> 5) * 24 + c4 * 8 + ((lane >> 4) & 1) * 4) = s4;
  }
  lds_barrier();
  STAMP(2);
  if (wave == NW - 1) {
    asm volatile("" : "+v"(ep));
    const u64 e1 = (((u64)ep[1] << 32) | ep[0]) + 1ull;
    const unsigned tA = (unsigned)e1, tB = (((unsigned)e1 * 0x9E3779B1u) ^ (unsigned)(e1 >> 32)) | 1u;
    if (lane < 12) {
      f32x4 a = {0.f, 0.f, 0.f, 0.f};
#pragma unroll
      for (int w = 0; w < NW; ++w) a += *reinterpret_cast<const f32x4*>(qp + w * 48 + lane * 4);
      put_unit(rs, W_QX + (unsigned)((h * 8 + sx) * 12 + lane) * 16, pack2(a[0], a[1]), pack2(a[2], a[3]), tA, tB);
    }
    if (lane == 0) { misc[0] = (NSLOT + 1) * NW; misc[1] = tA; misc[2] = tB; misc[3] = (unsigned)e1; misc[4] = (unsigned)(e1 >> 32); }
    const int li = lane < 48 ? lane : 0;
    unsigned off[1] = {W_QX + (unsigned)((h * 8 + li / 6) * 12 + b * 6 + li % 6) * 16};
    u32x4 v[1];
    get_units<1>(rs, off, v, lane < 48, tA, tB);
    if (lane < 48) *reinterpret_cast<u32x2*>(ql + 2 * lane) = u32x2{v[0][1], v[0][3]};
    asm volatile("s_waitcnt lgkmcnt(0)" ::: "memory");
    if (lane == 0) __hip_atomic_store(misc + 5, 1u, __ATOMIC_RELAXED, __HIP_MEMORY_SCOPE_WORKGROUP);
  }
  u32x4 wov[6];
  u32x4 w1v[5];
  unsigned w2v[24];
  unsigned bsm = 0;
  {
    WAITVMX(PD); dma_half(ring + 6144, kblk + (size_t)(wave + 8) * 3072, kvoff);
    WAITVMX(PD); dma_half(ring + 9216, vblk + (size_t)(wave + 8) * 3072, kvoff);
    WAITVMX(PD); dma_half(ring + 12288, kblk + (size_t)(wave + 16) * 3072, kvoff);
    WAITVMX(PD); dma_half(ring + 15360, vblk + (size_t)(wave + 16) * 3072, kvoff);
    const char* woc = reinterpret_cast<const char*>(wo) + (size_t)(h * DH + 24 * wave) * 1280 + sx * 160;
    const unsigned oa = (unsigned)(4 * rg) * 1280u + (c16 < 10 ? c16 : 9) * 16, ob = (unsigned)(16 + 2 * rg) * 1280u + (c16 < 10 ? c16 : 9) * 16;
    WAITVMX(PD); LD4NT(wov[0], oa, woc, 0); LD4NT(wov[1], oa, woc, 1280); LD4NT(wov[2], oa, woc, 2560);
    WAITVMX(PD); LD4NT(wov[3], oa, woc, 3840); LD4NT(wov[4], ob, woc, 0); LD4NT(wov[5], ob, woc, 1280);
    if (is_mlp) {
      const char* w1c = reinterpret_cast<const char*>(w1) + jb * 48;
      const unsigned wa = (unsigned)(80 * wave + 4 * g4) * 3072u + (c4 < 3 ? c4 : 2) * 16, wb = (unsigned)(80 * wave + 64 + g4) * 3072u + (c4 < 3 ? c4 : 2) * 16;
      WAITVMX(PD); LD4NT(w1v[0], wa, w1c, 0); LD4NT(w1v[1], wa, w1c, 3072); LD4NT(w1v[2], wa, w1c + 6144, 0);
      WAITVMX(PD); LD4NT(w1v[3], wa, w1c + 6144, 3072); LD4NT(w1v[4], wb, w1c, 0);
      const unsigned bo = (unsigned)(jb * 24 + (lane < 48 ? lane % 24 : 0)) * 2;
      LDU16(bsm, bo, b1, 0);
      const char* w2c = reinterpret_cast<const char*>(w2) + (size_t)jb * 30720;
      const unsigned o2 = (unsigned)(tid < 320 ? tid : 0) * 4;
      if (wave < 5) {
#pragma unroll
        for (int i = 0; i < 24; i += 12) {
          WAITVMX(PD);
#pragma unroll
          for (int k = 0; k < 12; k += 3) { LD1NT(w2v[i + k], o2, w2c + (i + k) * 1280, 0); LD1NT(w2v[i + k + 1], o2, w2c + (i + k) * 1280, 1280); LD1NT(w2v[i + k + 2], o2, w2c + (i + k) * 1280, 2560); }
        }
      }
    } else if (is_red && wave == 0) {
      const unsigned col = (unsigned)((rid % 40) * 16 + 2 * c + (lane >> 5));
      LDU16(w2v[0], col * 2, x + (rid / 40) * DM, 0);
      LDU16(w2v[1], col * 2, b2, 0);
    }
  }
  while (__hip_atomic_load(misc + 5, __ATOMIC_RELAXED, __HIP_MEMORY_SCOPE_WORKGROUP) == 0) __builtin_amdgcn_s_sleep(1);
  asm volatile("" ::: "memory");
  STAMP(3);
  const unsigned tA = misc[1], tB = misc[2];
  float m = -INFINITY, l = 0.f;
  float acc[24];
  unsigned wop[24], w1p[24];
  {
    u32x4 q[3];
#pragma unroll
    for (int t = 0; t < 3; ++t) q[t] = *reinterpret_cast<const u32x4*>(ql + t * 32 + c * 4);
#pragma unroll
    for (int k = 0; k < 24; ++k) acc[k] = 0.f;
    constexpr float SC = 0.07216878364870322f, L2E = 1.4426950408889634f;
    int nxt = NSLOT * NW + wave;
    int pend = NSLOT, slot = 0, it = 0;
    const unsigned char* rp = lds + L_RING + wave * RINGW + lane * 16;
    for (;; ++it) {
      if (pend == 3) asm volatile("s_waitcnt vmcnt(12)" ::: "memory");
      else if (pend == 2) asm volatile("s_waitcnt vmcnt(6)" ::: "memory");
      else asm volatile("s_waitcnt vmcnt(0)" ::: "memory");
      u32x4 kb[3], vb[3];
#pragma unroll
      for (int t = 0; t < 3; ++t) { kb[t] = *reinterpret_cast<const u32x4*>(rp + slot * 6144 + t * 1024); vb[t] = *reinterpret_cast<const u32x4*>(rp + slot * 6144 + 3072 + t * 1024); }
      if (nxt < nstep) {
        dma_step(ring + slot * 6144, kblk + (size_t)nxt * 3072, vblk + (size_t)nxt * 3072, kvoff);
        int n2 = 0;
        if (lane == 0) n2 = __hip_atomic_fetch_add(misc, 1u, __ATOMIC_RELAXED, __HIP_MEMORY_SCOPE_WORKGROUP);
        nxt = __builtin_amdgcn_readfirstlane(n2);
      } else {
        --pend;
      }
      float d = 0.f;
#pragma unroll
      for (int t = 0; t < 3; ++t)
#pragma unroll
        for (int k = 0; k < 4; ++k) d = dot2(kb[t][k], q[t][k], d);
      d = grp8_sum(d);
      const float s_ = rbf(rbf(d) * SC) * L2E;
      if (__any(s_ > m)) {
        const float mx = fmaxf(m, s_), al = __builtin_amdgcn_exp2f(m - mx);
        l *= al;
#pragma unroll
        for (int k = 0; k < 24; ++k) acc[k] *= al;
        m = mx;
      }
      const float p = __builtin_amdgcn_exp2f(s_ - m);
      l += p;
#pragma unroll
      for (int t = 0; t < 3; ++t)
#pragma unroll
        for (int k = 0; k < 4; ++k) {
          acc[t * 8 + 2 * k] = fmaf(p, bflo(vb[t][k]), acc[t * 8 + 2 * k]);
          acc[t * 8 + 2 * k + 1] = fmaf(p, bfhi(vb[t][k]), acc[t * 8 + 2 * k + 1]);
        }
      if (it == NSLOT) {
        asm volatile("; WEIGHTS_LANDED" : "+v"(wov[0]), "+v"(wov[1]), "+v"(wov[2]), "+v"(wov[3]), "+v"(wov[4]), "+v"(wov[5]), "+v"(w1v[0]), "+v"(w1v[1]), "+v"(w1v[2]), "+v"(w1v[3]), "+v"(w1v[4]), "+v"(bsm));
        asm volatile("; WEIGHTS_LANDED2" : "+v"(w2v[0]), "+v"(w2v[1]), "+v"(w2v[2]), "+v"(w2v[3]), "+v"(w2v[4]), "+v"(w2v[5]), "+v"(w2v[6]), "+v"(w2v[7]), "+v"(w2v[8]), "+v"(w2v[9]), "+v"(w2v[10]), "+v"(w2v[11]),
                     "+v"(w2v[12]), "+v"(w2v[13]), "+v"(w2v[14]), "+v"(w2v[15]), "+v"(w2v[16]), "+v"(w2v[17]), "+v"(w2v[18]), "+v"(w2v[19]), "+v"(w2v[20]), "+v"(w2v[21]), "+v"(w2v[22]), "+v"(w2v[23]));
#pragma unroll
        for (int pr = 0; pr < 3; ++pr)
#pragma unroll
          for (int k = 0; k < 4; ++k) { wop[pr * 8 + 2 * k] = plo(wov[2 * pr][k], wov[2 * pr + 1][k]); wop[pr * 8 + 2 * k + 1] = phi(wov[2 * pr][k], wov[2 * pr + 1][k]); }
        if (is_mlp) {
#pragma unroll
          for (int k = 0; k < 4; ++k) {
            w1p[2 * k] = plo(w1v[0][k], w1v[1][k]); w1p[2 * k + 1] = phi(w1v[0][k], w1v[1][k]);
            w1p[8 + 2 * k] = plo(w1v[2][k], w1v[3][k]); w1p[8 + 2 * k + 1] = phi(w1v[2][k], w1v[3][k]);
            w1p[16 + 2 * k] = w1v[4][k] & 0xffffu; w1p[16 + 2 * k + 1] = w1v[4][k] >> 16;
          }
          {
            float cs[8];
#pragma unroll
            for (int k = 0; k < 8; ++k) cs[k] = dot2(w1p[16 + k], 0x3f803f80u, dot2(w1p[8 + k], 0x3f803f80u, dot2(w1p[k], 0x3f803f80u, 0.f)));
            float c0 = sw16(sw32(cs[0], cs[4]), sw32(cs[2], cs[6])), c1 = sw16(sw32(cs[1], cs[5]), sw32(cs[3], cs[7]));
            c0 += dpp<ROR8>(c0); c0 += dpp<ROR4>(c0); c1 += dpp<ROR8>(c1); c1 += dpp<ROR4>(c1);
            if ((lane & 12) == 0 && c4 < 3) *reinterpret_cast<f32x2*>(lds + L_CS + (wave * 24 + c4 * 8 + 4 * (lane >> 5) + 2 * ((lane >> 4) & 1)) * 4) = f32x2{c0, c1};
          }
#pragma unroll
          for (int i = 0; i < 12; ++i) { const unsigned lo = plo(w2v[2 * i], w2v[2 * i + 1]), hi = phi(w2v[2 * i], w2v[2 * i + 1]); w2v[2 * i] = lo; w2v[2 * i + 1] = hi; }
        }
      }
      if (pend == 0) break;
      slot = slot == NSLOT - 1 ? 0 : slot + 1;
    }
    STAMP(4);
    const float M = xrow_max(fmaxf(m, dpp<ROR8>(m)));
    const float al = __builtin_amdgcn_exp2f(m - M);
    l *= al; l += dpp<ROR8>(l); l = sw16(l, l); l = sw32(l, l);
    float t12[12], t6[6];
#pragma unroll
    for (int k = 0; k < 12; ++k) t12[k] = sw32(acc[k] * al, acc[k + 12] * al);
#pragma unroll
    for (int k = 0; k < 6; ++k) { t6[k] = sw16(t12[k], t12[k + 6]); t6[k] += dpp<ROR8>(t6[k]); }
    float* rec = reinterpret_cast<float*>(lds + L_RING + wave * RINGW);
    if ((lane & 8) == 0) {
      float* p = rec + 4 + c * 24 + 12 * (lane >> 5) + 6 * ((lane >> 4) & 1);
#pragma unroll
      for (int k = 0; k < 3; ++k) *reinterpret_cast<f32x2*>(p + 2 * k) = f32x2{t6[2 * k], t6[2 * k + 1]};
      if (lane == 0) *reinterpret_cast<f32x2*>(rec) = f32x2{M, l};
    }
  }
  lds_barrier();
  if (tid < 97) {
    const int d = 2 * tid, idx = tid < 96 ? 4 + ((d & 63) >> 3) * 24 + (d >> 6) * 8 + (d & 7) : 0;
    f32x2 ml[NW], a[NW];
#pragma unroll
    for (int w = 0; w < NW; ++w) {
      const float* rec = reinterpret_cast<const float*>(lds + L_RING + w * RINGW);
      ml[w] = *reinterpret_cast<const f32x2*>(rec); a[w] = *reinterpret_cast<const f32x2*>(rec + idx);
    }
    float M = ml[0][0];
#pragma unroll
    for (int w = 1; w < NW; ++w) M = fmaxf(M, ml[w][0]);
    f32x2 o = {0.f, 0.f};
#pragma unroll
    for (int w = 0; w < NW; ++w) { const float wt = __builtin_amdgcn_exp2f(ml[w][0] - M); o = __builtin_elementwise_fma(f32x2{wt, wt}, a[w], o); }
    put_unit(rs, W_P1 + (unsigned)(h * 8 + sx) * 1792 + tid * 16, __float_as_uint(tid < 96 ? o[0] : M), __float_as_uint(o[1]), tA, tB);
  }
  if (wave < 3) {
    const int bb = tid >= 96, u = tid - 96 * bb;
    const unsigned base = W_P1 + (unsigned)(h * 8 + bb * 4) * 1792;
    u32x4 v[4], f[4];
#pragma unroll
    for (int jj = 0; jj < 4; ++jj) { f[jj] = __builtin_amdgcn_raw_buffer_load_b128(rs, base + jj * 1792 + 96 * 16, 0, 16); v[jj] = __builtin_amdgcn_raw_buffer_load_b128(rs, base + jj * 1792 + u * 16, 0, 16); }
#pragma nounroll
    for (unsigned spin = 0; spin < SPIN_CAP; ++spin) {
      bool okf[4], okv[4];
      int miss = 0;
      bool all = true;
#pragma unroll
      for (int jj = 0; jj < 4; ++jj) { okf[jj] = unit_ok(f[jj], tA, tB); okv[jj] = unit_ok(v[jj], tA, tB); miss += okf[jj] ? 0 : 1; all = all && okf[jj] && okv[jj]; }
      if (__all(all)) break;
      __builtin_amdgcn_s_sleep(1);
      asm volatile("" ::: "memory");
#pragma unroll
      for (int jj = 0; jj < 4; ++jj) {
        if (!okf[jj]) f[jj] = __builtin_amdgcn_raw_buffer_load_b128(rs, base + jj * 1792 + 96 * 16, 0, 16);
        if (!okv[jj] && (okf[jj] || miss <= 1)) v[jj] = __builtin_amdgcn_raw_buffer_load_b128(rs, base + jj * 1792 + u * 16, 0, 16);
      }
    }
    const float M = fmaxf(fmaxf(__uint_as_float(f[0][1]), __uint_as_float(f[1][1])), fmaxf(__uint_as_float(f[2][1]), __uint_as_float(f[3][1])));
    float L = 0.f, o0 = 0.f, o1 = 0.f;
#pragma unroll
    for (int jj = 0; jj < 4; ++jj) {
      const float wt = __builtin_amdgcn_exp2f(__uint_as_float(f[jj][1]) - M);
      L = fmaf(wt, __uint_as_float(f[jj][3]), L); o0 = fmaf(wt, __uint_as_float(v[jj][1]), o0); o1 = fmaf(wt, __uint_as_float(v[jj][3]), o1);
    }
    const float rl = __builtin_amdgcn_rcpf(L);
    t2[tid] = pack2(o0 * rl, o1 * rl);
  }
  lds_barrier();
  STAMP(5);
  {
    float a[16];
#pragma unroll
    for (int bb = 0; bb < 2; ++bb) {
      const u32x2 p01 = *reinterpret_cast<const u32x2*>(t2 + bb * 96 + 12 * wave + 2 * rg);
      const unsigned p2 = t2[bb * 96 + 12 * wave + 8 + rg];
#pragma unroll
      for (int k = 0; k < 8; ++k) a[bb * 8 + k] = dot2(wop[16 + k], p2, dot2(wop[8 + k], p01[1], dot2(wop[k], p01[0], 0.f)));
    }
    float t[8];
#pragma unroll
    for (int k = 0; k < 8; ++k) t[k] = sw32(a[k], a[8 + k]);
    const f32x4 s4 = {sw16(t[0], t[4]), sw16(t[1], t[5]), sw16(t[2], t[6]), sw16(t[3], t[7])};
    if (c16 < 10) *reinterpret_cast<f32x4*>(t1 + wave * 160 + (lane >> 5) * 80 + c16 * 8 + ((lane >> 4) & 1) * 4) = s4;
  }
  lds_barrier();
  if (tid < 80) {
    f32x2 a = {0.f, 0.f};
#pragma unroll
    for (int w = 0; w < NW; ++w) a += *reinterpret_cast<const f32x2*>(t1 + w * 160 + 2 * tid);
    put_unit(rs, W_Y2 + (unsigned)(h * 8 + sx) * 1280 + tid * 16, __float_as_uint(a[0]), __float_as_uint(a[1]), tA, tB);
  }
  STAMP(6);
  if (is_red) {
    if (wave == 0) {
      const int bq = rid / 40, cg = rid % 40;
      float hv;
      {
        unsigned off[4];
#pragma unroll
        for (int k = 0; k < 4; ++k) off[k] = W_Y2 + (unsigned)((4 * r + k) * 8 + cg / 5) * 1280 + (unsigned)(bq * 40 + (cg % 5) * 8 + c) * 16;
        u32x4 v[4];
        get_units<4>(rs, off, v, true, tA, tB);
        const float a0 = (__uint_as_float(v[0][1]) + __uint_as_float(v[1][1])) + (__uint_as_float(v[2][1]) + __uint_as_float(v[3][1]));
        const float a1 = (__uint_as_float(v[0][3]) + __uint_as_float(v[1][3])) + (__uint_as_float(v[2][3]) + __uint_as_float(v[3][3]));
        float s = sw32(a0, a1); s = sw16(s, s); s += dpp<ROR8>(s);
        hv = rbf(bf1(w2v[0]) + rbf(s));
        auto e = __builtin_amdgcn_permlane32_swap(__float_as_uint(hv), __float_as_uint(hv), false, false);
        const unsigned pk = (e[0] >> 16) | (e[1] & 0xffff0000u);
        const unsigned pn = (unsigned)__builtin_amdgcn_mov_dpp((int)pk, XOR1, 0xf, 0xf, true);
        if (lane < 8 && !(lane & 1)) put_unit(rs, W_H3 + (unsigned)(rid * 4 + (lane >> 1)) * 16, pk, pn, tA, tB);
      }
      STAMP(7);
      {
        unsigned off[8];
#pragma unroll
        for (int k = 0; k < 8; ++k) off[k] = W_O4 + (unsigned)(8 * r + k) * 10240 + (unsigned)(bq * 320 + cg * 8 + c) * 16;
        u32x4 v[8];
        get_units<8>(rs, off, v, true, tA, tB);
        float a0 = 0.f, a1 = 0.f;
#pragma unroll
        for (int k = 0; k < 8; ++k) { a0 += __uint_as_float(v[k][1]); a1 += __uint_as_float(v[k][3]); }
        float s = sw32(a0, a1); s = sw16(s, s); s += dpp<ROR8>(s);
        const float ov = rbf(hv + rbf(rbf(s) + bf1(w2v[1])));
        auto e = __builtin_amdgcn_permlane32_swap(__float_as_uint(ov), __float_as_uint(ov), false, false);
        if (lane < 8) reinterpret_cast<unsigned*>(out)[bq * 320 + cg * 8 + lane] = (e[0] >> 16) | (e[1] & 0xffff0000u);
        if (rid == 0 && lane == 0) __hip_atomic_store(reinterpret_cast<u64*>(ws + W_EPOCH), ((u64)misc[4] << 32) | misc[3], __ATOMIC_RELAXED, __HIP_MEMORY_SCOPE_AGENT);
      }
    }
  } else if (is_mlp) {
    const int li = lane < 48 ? lane : 0, lb = li >= 24;
    float csum = 0.f;
    if (wave < 5) {
#pragma unroll
      for (int w = 0; w < NW; ++w) csum += reinterpret_cast<const float*>(lds + L_CS)[w * 24 + li - 24 * lb];
      unsigned off[1] = {W_H3 + (unsigned)tid * 16};
      u32x4 v[1];
      get_units<1>(rs, off, v, true, tA, tB);
      *reinterpret_cast<u32x2*>(t2 + 2 * tid) = u32x2{v[0][1], v[0][3]};
      float s = dot2(v[0][1], 0x3f803f80u, dot2(v[0][3], 0x3f803f80u, 0.f));
      s = grp8_sum(s); s += dpp<MIR>(s); s = sw16(s, s);
      const float mu = s * (1.0f / 128.f);
      const f32x2 d0 = f32x2{bflo(v[0][1]), bfhi(v[0][1])} - mu, d1 = f32x2{bflo(v[0][3]), bfhi(v[0][3])} - mu;
      const f32x2 q2 = __builtin_elementwise_fma(d0, d0, d1 * d1);
      float q = q2[0] + q2[1];
      q = grp8_sum(q); q += dpp<MIR>(q); q = sw16(q, q);
      if ((lane & 31) == 0) *reinterpret_cast<f32x2*>(lds + L_ST + (tid >> 5) * 8) = f32x2{mu, q};
    }
    lds_barrier();
    STAMP(7);
    {
      float a[16];
#pragma unroll
      for (int bb = 0; bb < 2; ++bb) {
        const u32x2 ho = *reinterpret_cast<const u32x2*>(t2 + bb * 320 + 40 * wave + 2 * g4);
        const unsigned hs = reinterpret_cast<const u16*>(t2)[bb * 640 + 80 * wave + 64 + g4];
#pragma unroll
        for (int k = 0; k < 8; ++k) a[bb * 8 + k] = dot2(w1p[16 + k], hs, dot2(w1p[8 + k], ho[1], dot2(w1p[k], ho[0], 0.f)));
      }
      const f32x4 s4 = red16_g4(a);
      if ((lane & 12) == 0 && c4 < 3) *reinterpret_cast<f32x4*>(qp + wave * 48 + (lane >> 5) * 24 + c4 * 8 + ((lane >> 4) & 1) * 4) = s4;
    }
    float mb = 0.f, rd = 0.f;
    if (wave < 5) {
      f32x2 sg[5];
#pragma unroll
      for (int k = 0; k < 5; ++k) sg[k] = *reinterpret_cast<const f32x2*>(lds + L_ST + (lb * 5 + k) * 8);
      const float mean = ((sg[0][0] + sg[1][0]) + (sg[2][0] + sg[3][0]) + sg[4][0]) * 0.2f;
      float m2 = (sg[0][1] + sg[1][1]) + (sg[2][1] + sg[3][1]) + sg[4][1], dd = 0.f;
#pragma unroll
      for (int k = 0; k < 5; ++k) { const float d = sg[k][0] - mean; dd = fmaf(d, d, dd); }
      const float var = fmaf(dd, 128.f, m2) * (1.0f / DM);
      mb = rbf(mean);
      rd = __builtin_amdgcn_rcpf(rbf(__builtin_amdgcn_sqrtf(rbf(rbf(var) + 1e-5f))));
    }
    lds_barrier();
    if (wave < 5) {
      float s = 0.f;
#pragma unroll
      for (int w = 0; w < NW; ++w) s += qp[w * 48 + li];
      const float hv = rbf(rbf(fmaf(-mb, csum, s) * rd) + bf1(bsm));
      const float rr = hv > 0.f ? hv : 0.f;
      const float r2 = rr * rr;
      const unsigned rp2 = pack2(r2, dpp<XOR1>(r2));
      float a00 = 0.f, a01 = 0.f, a10 = 0.f, a11 = 0.f;
#pragma unroll
      for (int i = 0; i < 12; ++i) {
        const unsigned p0 = (unsigned)__builtin_amdgcn_readlane((int)rp2, 2 * i), p1 = (unsigned)__builtin_amdgcn_readlane((int)rp2, 24 + 2 * i);
        a00 = dot2(w2v[2 * i], p0, a00); a01 = dot2(w2v[2 * i + 1], p0, a01); a10 = dot2(w2v[2 * i], p1, a10); a11 = dot2(w2v[2 * i + 1], p1, a11);
      }
      put_unit(rs, W_O4 + (unsigned)jb * 10240 + (unsigned)tid * 16, __float_as_uint(a00), __float_as_uint(a01), tA, tB);
      put_unit(rs, W_O4 + (unsigned)jb * 10240 + (unsigned)(320 + tid) * 16, __float_as_uint(a10), __float_as_uint(a11), tA, tB);
    }
  }
  STAMP(8);
}

extern "C" void kernel_launch(void* const* d_in, const int* in_sizes, int n_in, void* d_out, int out_size, void* d_ws, size_t ws_size, hipStream_t stream) {
  fused_kernel<<<256, NTHR, 0, stream>>>((const u16*)d_in[0], (const u16*)d_in[1], (const u16*)d_in[2], (const u16*)d_in[3], (const u16*)d_in[4], (const u16*)d_in[6],
                                         (const u16*)d_in[7], (const u16*)d_in[8], (const u16*)d_in[9], (u16*)d_out, (unsigned char*)d_ws);
}
```

```cpp
#define STAMPS 1
#define PROBE 0
#include <hip/hip_runtime.h>
#include <hip/hip_bf16.h>
#include <stdint.h>

typedef unsigned u32x4 __attribute__((ext_vector_type(4)));
typedef unsigned u32x2 __attribute__((ext_vector_type(2)));
typedef float f32x4 __attribute__((ext_vector_type(4)));
typedef float f32x2 __attribute__((ext_vector_type(2)));
typedef __bf16 bf16x2 __attribute__((ext_vector_type(2)));
typedef unsigned short u16;
typedef unsigned long long u64;

#define PD 3

#define NSLOT 3
#define NS0 243
#define NS1 260
constexpr int DM = 640, NH = 32, SK = 8192, DH = 192, FF = 1536;
constexpr int NW = 8;
constexpr int NTHR = NW * 64;
constexpr int NRED = 80, NMLP = 64;
constexpr int RINGW = 18432;
constexpr int L_RING = 0;
constexpr int L_QP = NW * RINGW;
constexpr int L_Q = L_QP + 1536;
constexpr int L_MISC = L_Q + 384;
constexpr int L_T1 = L_MISC + 64;
constexpr int L_T2 = L_T1 + 5120;
constexpr int L_CS = L_T2 + 2560;
constexpr int L_ST = L_CS + 768;
constexpr int L_END = L_ST + 128;
static_assert(L_END <= 163840, "lds");
constexpr unsigned W_EPOCH = 0;
constexpr unsigned W_QX = 256;
constexpr unsigned W_P1 = 65536;
constexpr unsigned W_Y2 = W_P1 + 256 * 1792;
constexpr unsigned W_H3 = W_Y2 + 256 * 1280;
constexpr unsigned W_O4 = W_H3 + 5120;
constexpr unsigned W_END = W_O4 + 64 * 10240;
constexpr unsigned W_STAMP = 4u << 20;
constexpr unsigned SPIN_CAP = 400000;
constexpr int NSTAMP = 10;

__device__ __forceinline__ float rbf(float x) { return (float)(__bf16)x; }
__device__ __forceinline__ float bflo(unsigned v) { return __builtin_bit_cast(float, v << 16); }
__device__ __forceinline__ float bfhi(unsigned v) { return __builtin_bit_cast(float, v & 0xffff0000u); }
__device__ __forceinline__ float bf1(unsigned v) { return __builtin_bit_cast(float, v << 16); }
__device__ __forceinline__ unsigned pack2(float lo, float hi) { bf16x2 v = {(__bf16)lo, (__bf16)hi}; return __builtin_bit_cast(unsigned, v); }
__device__ __forceinline__ float dot2(unsigned k, unsigned q, float acc) {
  return __builtin_amdgcn_fdot2_f32_bf16(__builtin_bit_cast(bf16x2, k), __builtin_bit_cast(bf16x2, q), acc, false);
}
__device__ __forceinline__ unsigned plo(unsigned r0, unsigned r1) { return __builtin_amdgcn_perm(r1, r0, 0x05040100u); }
__device__ __forceinline__ unsigned phi(unsigned r0, unsigned r1) { return __builtin_amdgcn_perm(r1, r0, 0x07060302u); }
template <int CTRL> __device__ __forceinline__ float dpp(float x) {
  return __builtin_bit_cast(float, __builtin_amdgcn_mov_dpp(__builtin_bit_cast(int, x), CTRL, 0xf, 0xf, true));
}
constexpr int XOR1 = 0xB1, XOR2 = 0x4E, HMIR = 0x141, MIR = 0x140, ROR8 = 0x128, ROR4 = 0x124;
__device__ __forceinline__ float sw32(float a, float b) {
  auto s = __builtin_amdgcn_permlane32_swap(__float_as_uint(a), __float_as_uint(b), false, false);
  return __uint_as_float(s[0]) + __uint_as_float(s[1]);
}
__device__ __forceinline__ float sw16(float a, float b) {
  auto s = __builtin_amdgcn_permlane16_swap(__float_as_uint(a), __float_as_uint(b), false, false);
  return __uint_as_float(s[0]) + __uint_as_float(s[1]);
}
__device__ __forceinline__ float xrow_max(float x) {
  auto s = __builtin_amdgcn_permlane16_swap(__float_as_uint(x), __float_as_uint(x), false, false);
  x = fmaxf(__uint_as_float(s[0]), __uint_as_float(s[1]));
  auto t = __builtin_amdgcn_permlane32_swap(__float_as_uint(x), __float_as_uint(x), false, false);
  return fmaxf(__uint_as_float(t[0]), __uint_as_float(t[1]));
}
__device__ __forceinline__ float grp8_sum(float x) { x += dpp<XOR1>(x); x += dpp<XOR2>(x); x += dpp<HMIR>(x); return x; }
__device__ __forceinline__ void lds_barrier() { asm volatile("s_waitcnt lgkmcnt(0)" ::: "memory"); __builtin_amdgcn_s_barrier(); asm volatile("" ::: "memory"); }
__device__ __forceinline__ float rdlane(float x, int l) { return __builtin_bit_cast(float, __builtin_amdgcn_readlane(__builtin_bit_cast(int, x), l)); }

__device__ __forceinline__ f32x2 wave_sum2(float a, float b) {
  float t = sw32(a, b);
  t = sw16(t, t);
  t = grp8_sum(t);
  t += dpp<MIR>(t);
  return f32x2{rdlane(t, 0), rdlane(t, 32)};
}
__device__ __forceinline__ void ln_stats2(const unsigned (&r)[2][5], float (&mb)[2], float (&rd)[2]) {
  float s[2] = {0.f, 0.f};
#pragma unroll
  for (int i = 0; i < 5; ++i) { s[0] = dot2(r[0][i], 0x3f803f80u, s[0]); s[1] = dot2(r[1][i], 0x3f803f80u, s[1]); }
  const f32x2 mean = wave_sum2(s[0], s[1]) * (1.0f / DM);
  f32x2 q[2] = {{0.f, 0.f}, {0.f, 0.f}};
#pragma unroll
  for (int i = 0; i < 5; ++i)
#pragma unroll
    for (int bb = 0; bb < 2; ++bb) { const f32x2 d = f32x2{bflo(r[bb][i]), bfhi(r[bb][i])} - mean[bb]; q[bb] = __builtin_elementwise_fma(d, d, q[bb]); }
  const f32x2 var = wave_sum2(q[0][0] + q[0][1], q[1][0] + q[1][1]) * (1.0f / DM);
#pragma unroll
  for (int bb = 0; bb < 2; ++bb) {
    mb[bb] = rbf(mean[bb]);
    rd[bb] = __builtin_amdgcn_rcpf(rbf(__builtin_amdgcn_sqrtf(rbf(rbf(var[bb]) + 1e-5f))));
  }
}
__device__ __forceinline__ unsigned ln_pair(unsigned v, float mb, float rd) {
  const unsigned d = pack2(bflo(v) - mb, bfhi(v) - mb);
  return pack2(bflo(d) * rd, bfhi(d) * rd);
}
__device__ __forceinline__ unsigned ln_one(unsigned v, float mb, float rd) { return pack2(rbf(bf1(v) - mb) * rd, 0.f); }

__device__ __forceinline__ void dma_half(unsigned lds, const char* gb, unsigned voff) {
  unsigned keep;
  asm volatile(
      "s_mov_b32 %0, m0\n\t"
      "s_mov_b32 m0, %3\n\t"
      "s_nop 0\n\t"
      "global_load_lds_dwordx4 %1, %2 nt\n\t"
      "s_add_u32 m0, m0, 0x380\n\t"
      "s_nop 0\n\t"
      "global_load_lds_dwordx4 %1, %2 offset:128 nt\n\t"
      "s_add_u32 m0, m0, 0x380\n\t"
      "s_nop 0\n\t"
      "global_load_lds_dwordx4 %1, %2 offset:256 nt\n\t"
      "s_mov_b32 m0, %0"
      : "=&s"(keep) : "v"(voff), "s"(gb), "s"(lds) : "memory", "scc");
}
__device__ __forceinline__ void dma_step(unsigned lds, const char* kb, const char* vb, unsigned voff) {
  unsigned keep;
  asm volatile(
      "s_waitcnt lgkmcnt(0)\n\t"
      "s_mov_b32 %0, m0\n\t"
      "s_mov_b32 m0, %4\n\t"
      "s_nop 0\n\t"
      "global_load_lds_dwordx4 %1, %2 nt\n\t"
      "s_add_u32 m0, m0, 0x380\n\t"
      "s_nop 0\n\t"
      "global_load_lds_dwordx4 %1, %2 offset:128 nt\n\t"
      "s_add_u32 m0, m0, 0x380\n\t"
      "s_nop 0\n\t"
      "global_load_lds_dwordx4 %1, %2 offset:256 nt\n\t"
      "s_add_u32 m0, m0, 0x500\n\t"
      "s_nop 0\n\t"
      "global_load_lds_dwordx4 %1, %3 nt\n\t"
      "s_add_u32 m0, m0, 0x380\n\t"
      "s_nop 0\n\t"
      "global_load_lds_dwordx4 %1, %3 offset:128 nt\n\t"
      "s_add_u32 m0, m0, 0x380\n\t"
      "s_nop 0\n\t"
      "global_load_lds_dwordx4 %1, %3 offset:256 nt\n\t"
      "s_mov_b32 m0, %0"
      : "=&s"(keep) : "v"(voff), "s"(kb), "s"(vb), "s"(lds) : "memory", "scc");
}
#define LD4NT(dst, voff, base, off) asm volatile("global_load_dwordx4 %0, %1, %2 offset:%c3 nt" : "=v"(dst) : "v"(voff), "s"(base), "i"(off) : "memory")
#define LD1NT(dst, voff, base, off) asm volatile("global_load_dword %0, %1, %2 offset:%c3 nt" : "=v"(dst) : "v"(voff), "s"(base), "i"(off) : "memory")
#define LD4(dst, voff, base, off) asm volatile("global_load_dwordx4 %0, %1, %2 offset:%c3" : "=v"(dst) : "v"(voff), "s"(base), "i"(off) : "memory")
#define LD2(dst, voff, base, off) asm volatile("global_load_dwordx2 %0, %1, %2 offset:%c3" : "=v"(dst) : "v"(voff), "s"(base), "i"(off) : "memory")
#define LD1(dst, voff, base, off) asm volatile("global_load_dword %0, %1, %2 offset:%c3" : "=v"(dst) : "v"(voff), "s"(base), "i"(off) : "memory")
#define LDU16(dst, voff, base, off) asm volatile("global_load_ushort %0, %1, %2 offset:%c3" : "=v"(dst) : "v"(voff), "s"(base), "i"(off) : "memory")
#define WAITVM(n) asm volatile("s_waitcnt vmcnt(" #n ")" ::: "memory")
#define WAITVMX(n) WAITVM(n)

__device__ __forceinline__ void put_unit(__amdgpu_buffer_rsrc_t rs, unsigned off, unsigned v0, unsigned v1, unsigned tA, unsigned tB) {
  u32x4 u = {tA ^ v1, v0, tB ^ v0, v1};
  __builtin_amdgcn_raw_buffer_store_b128(u, rs, off, 0, 16);
}
__device__ __forceinline__ bool unit_ok(const u32x4& v, unsigned tA, unsigned tB) { return ((v[0] ^ v[3]) == tA) && ((v[2] ^ v[1]) == tB); }
template <int N>
__device__ __forceinline__ void get_units(__amdgpu_buffer_rsrc_t rs, const unsigned (&off)[N], u32x4 (&v)[N], bool active, unsigned tA, unsigned tB) {
  bool ok = !active;
#pragma nounroll
  for (unsigned spin = 0; spin < SPIN_CAP; ++spin) {
    if (!ok) {
      asm volatile("" ::: "memory");
#pragma unroll
      for (int k = 0; k < N; ++k) v[k] = __builtin_amdgcn_raw_buffer_load_b128(rs, off[k], 0, 16);
      bool g = true;
#pragma unroll
      for (int k = 0; k < N; ++k) g = g && unit_ok(v[k], tA, tB);
      ok = g;
    }
    if (__all(ok)) break;
    __builtin_amdgcn_s_sleep(1);
  }
}
__device__ __forceinline__ f32x4 red16_g4(const float (&a)[16]) {
  float t[8], u[4];
#pragma unroll
  for (int k = 0; k < 8; ++k) t[k] = sw32(a[k], a[8 + k]);
#pragma unroll
  for (int k = 0; k < 4; ++k) { u[k] = sw16(t[k], t[k + 4]); u[k] += dpp<ROR8>(u[k]); u[k] += dpp<ROR4>(u[k]); }
  return f32x4{u[0], u[1], u[2], u[3]};
}

__global__ __launch_bounds__(NTHR) void fused_kernel(const u16* __restrict__ x, const u16* __restrict__ Kc, const u16* __restrict__ Vc, const u16* __restrict__ wq,
                                                      const u16* __restrict__ wo, const u16* __restrict__ w1, const u16* __restrict__ b1, const u16* __restrict__ w2,
                                                      const u16* __restrict__ b2, u16* __restrict__ out, unsigned char* __restrict__ ws) {
  __shared__ __attribute__((aligned(16))) unsigned char lds[L_END];
  const int bid = blockIdx.x, loc = bid >> 3;
  const int h = (bid & 7) * 4 + (loc >> 3), sx = loc & 7, b = sx >> 2, j = sx & 3, g = b * NH + h;
  const int tid = threadIdx.x, lane = tid & 63, wave = __builtin_amdgcn_readfirstlane(tid >> 6), r = lane >> 3, c = lane & 7;
  const unsigned lds0 = __builtin_amdgcn_readfirstlane((unsigned)(uintptr_t)(__attribute__((address_space(3))) unsigned char*)lds);
  const __amdgpu_buffer_rsrc_t rs = __builtin_amdgcn_make_buffer_rsrc(ws, 0, W_END, 0x00020000);
  float* qp = reinterpret_cast<float*>(lds + L_QP);
  unsigned* ql = reinterpret_cast<unsigned*>(lds + L_Q);
  unsigned* misc = reinterpret_cast<unsigned*>(lds + L_MISC);
  float* t1 = reinterpret_cast<float*>(lds + L_T1);
  unsigned* t2 = reinterpret_cast<unsigned*>(lds + L_T2);
  u64 st[NSTAMP];
#pragma unroll
  for (int i = 0; i < NSTAMP; ++i) st[i] = 0;
#define STAMP(i) st[i] = __builtin_amdgcn_s_memrealtime()
  STAMP(0);
  const bool is_mlp = j == 0, is_red = j == 1 || (j == 2 && (g & 3) == 0);
  const int jb = g, rid = j == 1 ? g : 64 + (g >> 2);
  const int s0 = j == 0 ? 0 : NS0 + (j - 1) * NS1, nstep = j == 0 ? NS0 : (j == 3 ? 1024 - NS0 - 2 * NS1 : NS1);
  const char* kblk = reinterpret_cast<const char*>(Kc) + ((size_t)g * SK + (size_t)s0 * 8) * 384;
  const char* vblk = reinterpret_cast<const char*>(Vc) + ((size_t)g * SK + (size_t)s0 * 8) * 384;
  const unsigned ring = lds0 + L_RING + wave * RINGW;
  const unsigned kvoff = r * 384 + c * 16;
  const int g4 = lane >> 2, c4 = lane & 3, rg = lane >> 4, c16 = lane & 15;
  u32x2 ep;
  if (wave == NW - 1) {
    asm volatile("global_load_dwordx2 %0, %1, %2 sc1" : "=v"(ep) : "v"(0u), "s"(ws) : "memory");
    if (lane == 0) misc[5] = 0;
  }
  {
    u32x4 xa[2], wv[5];
    unsigned xb[2], xs[2];
    u32x2 xo[2];
    const unsigned o16 = lane * 16, o4 = lane * 4, oo = (80 * wave + 4 * g4) * 2, os = (80 * wave + 64 + g4) * 2;
    LD4(xa[0], o16, x, 0); LD1(xb[0], o4, x, 1024); LD4(xa[1], o16, x, 1280); LD1(xb[1], o4, x, 2304);
    LD2(xo[0], oo, x, 0); LDU16(xs[0], os, x, 0); LD2(xo[1], oo, x, 1280); LDU16(xs[1], os, x, 1280);
    const unsigned wcol = (unsigned)(h * 384 + sx * 48 + (c4 < 3 ? c4 : 2) * 16);
    const unsigned wa = (unsigned)(80 * wave + 4 * g4) * 12288u + wcol, wb = (unsigned)(80 * wave + 64 + g4) * 12288u + wcol;
    const char* wqc = reinterpret_cast<const char*>(wq);
    LD4NT(wv[0], wa, wqc, 0); LD4NT(wv[1], wa, wqc + 12288, 0); LD4NT(wv[2], wa, wqc + 24576, 0); LD4NT(wv[3], wa, wqc + 36864, 0); LD4NT(wv[4], wb, wqc, 0);
    dma_half(ring, kblk + (size_t)wave * 3072, kvoff);
    dma_half(ring + 3072, vblk + (size_t)wave * 3072, kvoff);
    asm volatile("s_waitcnt vmcnt(11)" : "+v"(xa[0]), "+v"(xa[1]), "+v"(xb[0]), "+v"(xb[1]), "+v"(xo[0]), "+v"(xo[1]), "+v"(xs[0]), "+v"(xs[1])::"memory");
    const unsigned xr[2][5] = {{xa[0][0], xa[0][1], xa[0][2], xa[0][3], xb[0]}, {xa[1][0], xa[1][1], xa[1][2], xa[1][3], xb[1]}};
    float mb[2], rd[2];
    ln_stats2(xr, mb, rd);
    unsigned xp[2][3];
#pragma unroll
    for (int bb = 0; bb < 2; ++bb) { xp[bb][0] = ln_pair(xo[bb][0], mb[bb], rd[bb]); xp[bb][1] = ln_pair(xo[bb][1], mb[bb], rd[bb]); xp[bb][2] = ln_one(xs[bb], mb[bb], rd[bb]); }
    asm volatile("s_waitcnt vmcnt(6)" : "+v"(wv[0]), "+v"(wv[1]), "+v"(wv[2]), "+v"(wv[3]), "+v"(wv[4]), "+v"(xp[0][0]), "+v"(xp[0][1]), "+v"(xp[0][2]), "+v"(xp[1][0]), "+v"(xp[1][1]), "+v"(xp[1][2])::"memory");
    STAMP(1);
    float a[16];
#pragma unroll
    for (int k = 0; k < 4; ++k) {
      const unsigned p0 = plo(wv[0][k], wv[1][k]), p1 = phi(wv[0][k], wv[1][k]), p2 = plo(wv[2][k], wv[3][k]), p3 = phi(wv[2][k], wv[3][k]);
      const unsigned p4 = wv[4][k] & 0xffffu, p5 = wv[4][k] >> 16;
#pragma unroll
      for (int bb = 0; bb < 2; ++bb) {
        a[bb * 8 + 2 * k] = dot2(p4, xp[bb][2], dot2(p2, xp[bb][1], dot2(p0, xp[bb][0], 0.f)));
        a[bb * 8 + 2 * k + 1] = dot2(p5, xp[bb][2], dot2(p3, xp[bb][1], dot2(p1, xp[bb][0], 0.f)));
      }
    }
    const f32x4 s4 = red16_g4(a);
    if ((lane & 12) == 0 && c4 < 3) *reinterpret_cast<f32x4*>(qp + wave * 48 + (lane >> 5) * 24 + c4 * 8 + ((lane >> 4) & 1) * 4) = s4;
  }
  lds_barrier();
  STAMP(2);
  if (wave == NW - 1) {
    asm volatile("" : "+v"(ep));
    const u64 e1 = (((u64)ep[1] << 32) | ep[0]) + 1ull;
    const unsigned tA = (unsigned)e1, tB = (((unsigned)e1 * 0x9E3779B1u) ^ (unsigned)(e1 >> 32)) | 1u;
    if (lane < 12) {
      f32x4 a = {0.f, 0.f, 0.f, 0.f};
#pragma unroll
      for (int w = 0; w < NW; ++w) a += *reinterpret_cast<const f32x4*>(qp + w * 48 + lane * 4);
      put_unit(rs, W_QX + (unsigned)((h * 8 + sx) * 12 + lane) * 16, pack2(a[0], a[1]), pack2(a[2], a[3]), tA, tB);
    }
    if (lane == 0) { misc[0] = (NSLOT + 1) * NW; misc[1] = tA; misc[2] = tB; misc[3] = (unsigned)e1; misc[4] = (unsigned)(e1 >> 32); }
    const int li = lane < 48 ? lane : 0;
    unsigned off[1] = {W_QX + (unsigned)((h * 8 + li / 6) * 12 + b * 6 + li % 6) * 16};
    u32x4 v[1];
    get_units<1>(rs, off, v, lane < 48, tA, tB);
    if (lane < 48) *reinterpret_cast<u32x2*>(ql + 2 * lane) = u32x2{v[0][1], v[0][3]};
    asm volatile("s_waitcnt lgkmcnt(0)" ::: "memory");
    if (lane == 0) __hip_atomic_store(misc + 5, 1u, __ATOMIC_RELAXED, __HIP_MEMORY_SCOPE_WORKGROUP);
    STAMP(9);
  }
  u32x4 wov[6];
  u32x4 w1v[5];
  unsigned w2v[24];
  unsigned bsm = 0;
  {
    WAITVMX(PD); dma_half(ring + 6144, kblk + (size_t)(wave + 8) * 3072, kvoff);
    WAITVMX(PD); dma_half(ring + 9216, vblk + (size_t)(wave + 8) * 3072, kvoff);
    WAITVMX(PD); dma_half(ring + 12288, kblk + (size_t)(wave + 16) * 3072, kvoff);
    WAITVMX(PD); dma_half(ring + 15360, vblk + (size_t)(wave + 16) * 3072, kvoff);
    const char* woc = reinterpret_cast<const char*>(wo) + (size_t)(h * DH + 24 * wave) * 1280 + sx * 160;
    const unsigned oa = (unsigned)(4 * rg) * 1280u + (c16 < 10 ? c16 : 9) * 16, ob = (unsigned)(16 + 2 * rg) * 1280u + (c16 < 10 ? c16 : 9) * 16;
    WAITVMX(PD); LD4NT(wov[0], oa, woc, 0); LD4NT(wov[1], oa, woc, 1280); LD4NT(wov[2], oa, woc, 2560);
    WAITVMX(PD); LD4NT(wov[3], oa, woc, 3840); LD4NT(wov[4], ob, woc, 0); LD4NT(wov[5], ob, woc, 1280);
    if (is_mlp) {
      const char* w1c = reinterpret_cast<const char*>(w1) + jb * 48;
      const unsigned wa = (unsigned)(80 * wave + 4 * g4) * 3072u + (c4 < 3 ? c4 : 2) * 16, wb = (unsigned)(80 * wave + 64 + g4) * 3072u + (c4 < 3 ? c4 : 2) * 16;
      WAITVMX(PD); LD4NT(w1v[0], wa, w1c, 0); LD4NT(w1v[1], wa, w1c, 3072); LD4NT(w1v[2], wa, w1c + 6144, 0);
      WAITVMX(PD); LD4NT(w1v[3], wa, w1c + 6144, 3072); LD4NT(w1v[4], wb, w1c, 0);
      const unsigned bo = (unsigned)(jb * 24 + (lane < 48 ? lane % 24 : 0)) * 2;
      LDU16(bsm, bo, b1, 0);
      const char* w2c = reinterpret_cast<const char*>(w2) + (size_t)jb * 30720;
      const unsigned o2 = (unsigned)(tid < 320 ? tid : 0) * 4;
      if (wave < 5) {
#pragma unroll
        for (int i = 0; i < 24; i += 12) {
          WAITVMX(PD);
#pragma unroll
          for (int k = 0; k < 12; k += 3) { LD1NT(w2v[i + k], o2, w2c + (i + k) * 1280, 0); LD1NT(w2v[i + k + 1], o2, w2c + (i + k) * 1280, 1280); LD1NT(w2v[i + k + 2], o2, w2c + (i + k) * 1280, 2560); }
        }
      }
    } else if (is_red && wave == 0) {
      const unsigned col = (unsigned)((rid % 40) * 16 + 2 * c + (lane >> 5));
      LDU16(w2v[0], col * 2, x + (rid / 40) * DM, 0);
      LDU16(w2v[1], col * 2, b2, 0);
    }
  }
  while (__hip_atomic_load(misc + 5, __ATOMIC_RELAXED, __HIP_MEMORY_SCOPE_WORKGROUP) == 0) __builtin_amdgcn_s_sleep(1);
  asm volatile("" ::: "memory");
  STAMP(3);
  const unsigned tA = misc[1], tB = misc[2];
  float m = -INFINITY, l = 0.f;
  float acc[24];
  unsigned wop[24], w1p[24];
  {
    u32x4 q[3];
#pragma unroll
    for (int t = 0; t < 3; ++t) q[t] = *reinterpret_cast<const u32x4*>(ql + t * 32 + c * 4);
#pragma unroll
    for (int k = 0; k < 24; ++k) acc[k] = 0.f;
    constexpr float SC = 0.07216878364870322f, L2E = 1.4426950408889634f;
    int nxt = NSLOT * NW + wave;
    int pend = NSLOT, slot = 0, it = 0;
    const unsigned char* rp = lds + L_RING + wave * RINGW + lane * 16;
    for (;; ++it) {
      if (pend == 3) asm volatile("s_waitcnt vmcnt(12)" ::: "memory");
      else if (pend == 2) asm volatile("s_waitcnt vmcnt(6)" ::: "memory");
      else asm volatile("s_waitcnt vmcnt(0)" ::: "memory");
      u32x4 kb[3], vb[3];
#pragma unroll
      for (int t = 0; t < 3; ++t) { kb[t] = *reinterpret_cast<const u32x4*>(rp + slot * 6144 + t * 1024); vb[t] = *reinterpret_cast<const u32x4*>(rp + slot * 6144 + 3072 + t * 1024); }
      if (nxt < nstep) {
        dma_step(ring + slot * 6144, kblk + (size_t)nxt * 3072, vblk + (size_t)nxt * 3072, kvoff);
        int n2 = 0;
        if (lane == 0) n2 = __hip_atomic_fetch_add(misc, 1u, __ATOMIC_RELAXED, __HIP_MEMORY_SCOPE_WORKGROUP);
        nxt = __builtin_amdgcn_readfirstlane(n2);
      } else {
        --pend;
      }
      float d = 0.f;
#pragma unroll
      for (int t = 0; t < 3; ++t)
#pragma unroll
        for (int k = 0; k < 4; ++k) d = dot2(kb[t][k], q[t][k], d);
      d = grp8_sum(d);
      const float s_ = rbf(rbf(d) * SC) * L2E;
      if (__any(s_ > m)) {
        const float mx = fmaxf(m, s_), al = __builtin_amdgcn_exp2f(m - mx);
        l *= al;
#pragma unroll
        for (int k = 0; k < 24; ++k) acc[k] *= al;
        m = mx;
      }
      const float p = __builtin_amdgcn_exp2f(s_ - m);
      l += p;
#pragma unroll
      for (int t = 0; t < 3; ++t)
#pragma unroll
        for (int k = 0; k < 4; ++k) {
          acc[t * 8 + 2 * k] = fmaf(p, bflo(vb[t][k]), acc[t * 8 + 2 * k]);
          acc[t * 8 + 2 * k + 1] = fmaf(p, bfhi(vb[t][k]), acc[t * 8 + 2 * k + 1]);
        }
      if (it == NSLOT) {
        asm volatile("; WEIGHTS_LANDED" : "+v"(wov[0]), "+v"(wov[1]), "+v"(wov[2]), "+v"(wov[3]), "+v"(wov[4]), "+v"(wov[5]), "+v"(w1v[0]), "+v"(w1v[1]), "+v"(w1v[2]), "+v"(w1v[3]), "+v"(w1v[4]), "+v"(bsm));
        asm volatile("; WEIGHTS_LANDED2" : "+v"(w2v[0]), "+v"(w2v[1]), "+v"(w2v[2]), "+v"(w2v[3]), "+v"(w2v[4]), "+v"(w2v[5]), "+v"(w2v[6]), "+v"(w2v[7]), "+v"(w2v[8]), "+v"(w2v[9]), "+v"(w2v[10]), "+v"(w2v[11]),
                     "+v"(w2v[12]), "+v"(w2v[13]), "+v"(w2v[14]), "+v"(w2v[15]), "+v"(w2v[16]), "+v"(w2v[17]), "+v"(w2v[18]), "+v"(w2v[19]), "+v"(w2v[20]), "+v"(w2v[21]), "+v"(w2v[22]), "+v"(w2v[23]));
#pragma unroll
        for (int pr = 0; pr < 3; ++pr)
#pragma unroll
          for (int k = 0; k < 4; ++k) { wop[pr * 8 + 2 * k] = plo(wov[2 * pr][k], wov[2 * pr + 1][k]); wop[pr * 8 + 2 * k + 1] = phi(wov[2 * pr][k], wov[2 * pr + 1][k]); }
        if (is_mlp) {
#pragma unroll
          for (int k = 0; k < 4; ++k) {
            w1p[2 * k] = plo(w1v[0][k], w1v[1][k]); w1p[2 * k + 1] = phi(w1v[0][k], w1v[1][k]);
            w1p[8 + 2 * k] = plo(w1v[2][k], w1v[3][k]); w1p[8 + 2 * k + 1] = phi(w1v[2][k], w1v[3][k]);
            w1p[16 + 2 * k] = w1v[4][k] & 0xffffu; w1p[16 + 2 * k + 1] = w1v[4][k] >> 16;
          }
          {
            float cs[8];
#pragma unroll
            for (int k = 0; k < 8; ++k) cs[k] = dot2(w1p[16 + k], 0x3f803f80u, dot2(w1p[8 + k], 0x3f803f80u, dot2(w1p[k], 0x3f803f80u, 0.f)));
            float c0 = sw16(sw32(cs[0], cs[4]), sw32(cs[2], cs[6])), c1 = sw16(sw32(cs[1], cs[5]), sw32(cs[3], cs[7]));
            c0 += dpp<ROR8>(c0); c0 += dpp<ROR4>(c0); c1 += dpp<ROR8>(c1); c1 += dpp<ROR4>(c1);
            if ((lane & 12) == 0 && c4 < 3) *reinterpret_cast<f32x2*>(lds + L_CS + (wave * 24 + c4 * 8 + 4 * (lane >> 5) + 2 * ((lane >> 4) & 1)) * 4) = f32x2{c0, c1};
          }
#pragma unroll
          for (int i = 0; i < 12; ++i) { const unsigned lo = plo(w2v[2 * i], w2v[2 * i + 1]), hi = phi(w2v[2 * i], w2v[2 * i + 1]); w2v[2 * i] = lo; w2v[2 * i + 1] = hi; }
        }
      }
      if (pend == 0) break;
      slot = slot == NSLOT - 1 ? 0 : slot + 1;
    }
    STAMP(4);
    const float M = xrow_max(fmaxf(m, dpp<ROR8>(m)));
    const float al = __builtin_amdgcn_exp2f(m - M);
    l *= al; l += dpp<ROR8>(l); l = sw16(l, l); l = sw32(l, l);
    float t12[12], t6[6];
#pragma unroll
    for (int k = 0; k < 12; ++k) t12[k] = sw32(acc[k] * al, acc[k + 12] * al);
#pragma unroll
    for (int k = 0; k < 6; ++k) { t6[k] = sw16(t12[k], t12[k + 6]); t6[k] += dpp<ROR8>(t6[k]); }
    float* rec = reinterpret_cast<float*>(lds + L_RING + wave * RINGW);
    if ((lane & 8) == 0) {
      float* p = rec + 4 + c * 24 + 12 * (lane >> 5) + 6 * ((lane >> 4) & 1);
#pragma unroll
      for (int k = 0; k < 3; ++k) *reinterpret_cast<f32x2*>(p + 2 * k) = f32x2{t6[2 * k], t6[2 * k + 1]};
      if (lane == 0) *reinterpret_cast<f32x2*>(rec) = f32x2{M, l};
    }
  }
  lds_barrier();
  if (tid < 97) {
    const int d = 2 * tid, idx = tid < 96 ? 4 + ((d & 63) >> 3) * 24 + (d >> 6) * 8 + (d & 7) : 0;
    f32x2 ml[NW], a[NW];
#pragma unroll
    for (int w = 0; w < NW; ++w) {
      const float* rec = reinterpret_cast<const float*>(lds + L_RING + w * RINGW);
      ml[w] = *reinterpret_cast<const f32x2*>(rec); a[w] = *reinterpret_cast<const f32x2*>(rec + idx);
    }
    float M = ml[0][0];
#pragma unroll
    for (int w = 1; w < NW; ++w) M = fmaxf(M, ml[w][0]);
    f32x2 o = {0.f, 0.f};
#pragma unroll
    for (int w = 0; w < NW; ++w) { const float wt = __builtin_amdgcn_exp2f(ml[w][0] - M); o = __builtin_elementwise_fma(f32x2{wt, wt}, a[w], o); }
    put_unit(rs, W_P1 + (unsigned)(h * 8 + sx) * 1792 + tid * 16, __float_as_uint(tid < 96 ? o[0] : M), __float_as_uint(o[1]), tA, tB);
  }
  if (wave < 3) {
    const int bb = tid >= 96, u = tid - 96 * bb;
    const unsigned base = W_P1 + (unsigned)(h * 8 + bb * 4) * 1792;
    u32x4 v[4], f[4];
#pragma unroll
    for (int jj = 0; jj < 4; ++jj) { f[jj] = __builtin_amdgcn_raw_buffer_load_b128(rs, base + jj * 1792 + 96 * 16, 0, 16); v[jj] = __builtin_amdgcn_raw_buffer_load_b128(rs, base + jj * 1792 + u * 16, 0, 16); }
#pragma nounroll
    for (unsigned spin = 0; spin < SPIN_CAP; ++spin) {
      bool okf[4], okv[4];
      int miss = 0;
      bool all = true;
#pragma unroll
      for (int jj = 0; jj < 4; ++jj) { okf[jj] = unit_ok(f[jj], tA, tB); okv[jj] = unit_ok(v[jj], tA, tB); miss += okf[jj] ? 0 : 1; all = all && okf[jj] && okv[jj]; }
      if (__all(all)) break;
      __builtin_amdgcn_s_sleep(1);
      asm volatile("" ::: "memory");
#pragma unroll
      for (int jj = 0; jj < 4; ++jj) {
        if (!okf[jj]) f[jj] = __builtin_amdgcn_raw_buffer_load_b128(rs, base + jj * 1792 + 96 * 16, 0, 16);
        if (!okv[jj] && (okf[jj] || miss <= 1)) v[jj] = __builtin_amdgcn_raw_buffer_load_b128(rs, base + jj * 1792 + u * 16, 0, 16);
      }
    }
    const float M = fmaxf(fmaxf(__uint_as_float(f[0][1]), __uint_as_float(f[1][1])), fmaxf(__uint_as_float(f[2][1]), __uint_as_float(f[3][1])));
    float L = 0.f, o0 = 0.f, o1 = 0.f;
#pragma unroll
    for (int jj = 0; jj < 4; ++jj) {
      const float wt = __builtin_amdgcn_exp2f(__uint_as_float(f[jj][1]) - M);
      L = fmaf(wt, __uint_as_float(f[jj][3]), L); o0 = fmaf(wt, __uint_as_float(v[jj][1]), o0); o1 = fmaf(wt, __uint_as_float(v[jj][3]), o1);
    }
    const float rl = __builtin_amdgcn_rcpf(L);
    t2[tid] = pack2(o0 * rl, o1 * rl);
  }
  lds_barrier();
  STAMP(5);
  {
    float a[16];
#pragma unroll
    for (int bb = 0; bb < 2; ++bb) {
      const u32x2 p01 = *reinterpret_cast<const u32x2*>(t2 + bb * 96 + 12 * wave + 2 * rg);
      const unsigned p2 = t2[bb * 96 + 12 * wave + 8 + rg];
#pragma unroll
      for (int k = 0; k < 8; ++k) a[bb * 8 + k] = dot2(wop[16 + k], p2, dot2(wop[8 + k], p01[1], dot2(wop[k], p01[0], 0.f)));
    }
    float t[8];
#pragma unroll
    for (int k = 0; k < 8; ++k) t[k] = sw32(a[k], a[8 + k]);
    const f32x4 s4 = {sw16(t[0], t[4]), sw16(t[1], t[5]), sw16(t[2], t[6]), sw16(t[3], t[7])};
    if (c16 < 10) *reinterpret_cast<f32x4*>(t1 + wave * 160 + (lane >> 5) * 80 + c16 * 8 + ((lane >> 4) & 1) * 4) = s4;
  }
  lds_barrier();
  if (tid < 80) {
    f32x2 a = {0.f, 0.f};
#pragma unroll
    for (int w = 0; w < NW; ++w) a += *reinterpret_cast<const f32x2*>(t1 + w * 160 + 2 * tid);
    put_unit(rs, W_Y2 + (unsigned)(h * 8 + sx) * 1280 + tid * 16, __float_as_uint(a[0]), __float_as_uint(a[1]), tA, tB);
  }
  STAMP(6);
  if (is_red) {
    if (wave == 0) {
      const int bq = rid / 40, cg = rid % 40;
      float hv;
      {
        unsigned off[4];
#pragma unroll
        for (int k = 0; k < 4; ++k) off[k] = W_Y2 + (unsigned)((4 * r + k) * 8 + cg / 5) * 1280 + (unsigned)(bq * 40 + (cg % 5) * 8 + c) * 16;
        u32x4 v[4];
        get_units<4>(rs, off, v, true, tA, tB);
        const float a0 = (__uint_as_float(v[0][1]) + __uint_as_float(v[1][1])) + (__uint_as_float(v[2][1]) + __uint_as_float(v[3][1]));
        const float a1 = (__uint_as_float(v[0][3]) + __uint_as_float(v[1][3])) + (__uint_as_float(v[2][3]) + __uint_as_float(v[3][3]));
        float s = sw32(a0, a1); s = sw16(s, s); s += dpp<ROR8>(s);
        hv = rbf(bf1(w2v[0]) + rbf(s));
        auto e = __builtin_amdgcn_permlane32_swap(__float_as_uint(hv), __float_as_uint(hv), false, false);
        const unsigned pk = (e[0] >> 16) | (e[1] & 0xffff0000u);
        const unsigned pn = (unsigned)__builtin_amdgcn_mov_dpp((int)pk, XOR1, 0xf, 0xf, true);
        if (lane < 8 && !(lane & 1)) put_unit(rs, W_H3 + (unsigned)(rid * 4 + (lane >> 1)) * 16, pk, pn, tA, tB);
      }
      STAMP(7);
      {
        unsigned off[8];
#pragma unroll
        for (int k = 0; k < 8; ++k) off[k] = W_O4 + (unsigned)(8 * r + k) * 10240 + (unsigned)(bq * 320 + cg * 8 + c) * 16;
        u32x4 v[8];
        get_units<8>(rs, off, v, true, tA, tB);
        float a0 = 0.f, a1 = 0.f;
#pragma unroll
        for (int k = 0; k < 8; ++k) { a0 += __uint_as_float(v[k][1]); a1 += __uint_as_float(v[k][3]); }
        float s = sw32(a0, a1); s = sw16(s, s); s += dpp<ROR8>(s);
        const float ov = rbf(hv + rbf(rbf(s) + bf1(w2v[1])));
        auto e = __builtin_amdgcn_permlane32_swap(__float_as_uint(ov), __float_as_uint(ov), false, false);
        if (lane < 8) reinterpret_cast<unsigned*>(out)[bq * 320 + cg * 8 + lane] = (e[0] >> 16) | (e[1] & 0xffff0000u);
        if (rid == 0 && lane == 0) __hip_atomic_store(reinterpret_cast<u64*>(ws + W_EPOCH), ((u64)misc[4] << 32) | misc[3], __ATOMIC_RELAXED, __HIP_MEMORY_SCOPE_AGENT);
      }
    }
  } else if (is_mlp) {
    const int li = lane < 48 ? lane : 0, lb = li >= 24;
    float csum = 0.f;
    if (wave < 5) {
#pragma unroll
      for (int w = 0; w < NW; ++w) csum += reinterpret_cast<const float*>(lds + L_CS)[w * 24 + li - 24 * lb];
      unsigned off[1] = {W_H3 + (unsigned)tid * 16};
      u32x4 v[1];
      get_units<1>(rs, off, v, true, tA, tB);
      *reinterpret_cast<u32x2*>(t2 + 2 * tid) = u32x2{v[0][1], v[0][3]};
      float s = dot2(v[0][1], 0x3f803f80u, dot2(v[0][3], 0x3f803f80u, 0.f));
      s = grp8_sum(s); s += dpp<MIR>(s); s = sw16(s, s);
      const float mu = s * (1.0f / 128.f);
      const f32x2 d0 = f32x2{bflo(v[0][1]), bfhi(v[0][1])} - mu, d1 = f32x2{bflo(v[0][3]), bfhi(v[0][3])} - mu;
      const f32x2 q2 = __builtin_elementwise_fma(d0, d0, d1 * d1);
      float q = q2[0] + q2[1];
      q = grp8_sum(q); q += dpp<MIR>(q); q = sw16(q, q);
      if ((lane & 31) == 0) *reinterpret_cast<f32x2*>(lds + L_ST + (tid >> 5) * 8) = f32x2{mu, q};
    }
    lds_barrier();
    STAMP(7);
    {
      float a[16];
#pragma unroll
      for (int bb = 0; bb < 2; ++bb) {
        const u32x2 ho = *reinterpret_cast<const u32x2*>(t2 + bb * 320 + 40 * wave + 2 * g4);
        const unsigned hs = reinterpret_cast<const u16*>(t2)[bb * 640 + 80 * wave + 64 + g4];
#pragma unroll
        for (int k = 0; k < 8; ++k) a[bb * 8 + k] = dot2(w1p[16 + k], hs, dot2(w1p[8 + k], ho[1], dot2(w1p[k], ho[0], 0.f)));
      }
      const f32x4 s4 = red16_g4(a);
      if ((lane & 12) == 0 && c4 < 3) *reinterpret_cast<f32x4*>(qp + wave * 48 + (lane >> 5) * 24 + c4 * 8 + ((lane >> 4) & 1) * 4) = s4;
    }
    float mb = 0.f, rd = 0.f;
    if (wave < 5) {
      f32x2 sg[5];
#pragma unroll
      for (int k = 0; k < 5; ++k) sg[k] = *reinterpret_cast<const f32x2*>(lds + L_ST + (lb * 5 + k) * 8);
      const float mean = ((sg[0][0] + sg[1][0]) + (sg[2][0] + sg[3][0]) + sg[4][0]) * 0.2f;
      float m2 = (sg[0][1] + sg[1][1]) + (sg[2][1] + sg[3][1]) + sg[4][1], dd = 0.f;
#pragma unroll
      for (int k = 0; k < 5; ++k) { const float d = sg[k][0] - mean; dd = fmaf(d, d, dd); }
      const float var = fmaf(dd, 128.f, m2) * (1.0f / DM);
      mb = rbf(mean);
      rd = __builtin_amdgcn_rcpf(rbf(__builtin_amdgcn_sqrtf(rbf(rbf(var) + 1e-5f))));
    }
    lds_barrier();
    if (wave < 5) {
      float s = 0.f;
#pragma unroll
      for (int w = 0; w < NW; ++w) s += qp[w * 48 + li];
      const float hv = rbf(rbf(fmaf(-mb, csum, s) * rd) + bf1(bsm));
      const float rr = hv > 0.f ? hv : 0.f;
      const float r2 = rr * rr;
      const unsigned rp2 = pack2(r2, dpp<XOR1>(r2));
      float a00 = 0.f, a01 = 0.f, a10 = 0.f, a11 = 0.f;
#pragma unroll
      for (int i = 0; i < 12; ++i) {
        const unsigned p0 = (unsigned)__builtin_amdgcn_readlane((int)rp2, 2 * i), p1 = (unsigned)__builtin_amdgcn_readlane((int)rp2, 24 + 2 * i);
        a00 = dot2(w2v[2 * i], p0, a00); a01 = dot2(w2v[2 * i + 1], p0, a01); a10 = dot2(w2v[2 * i], p1, a10); a11 = dot2(w2v[2 * i + 1], p1, a11);
      }
      put_unit(rs, W_O4 + (unsigned)jb * 10240 + (unsigned)tid * 16, __float_as_uint(a00), __float_as_uint(a01), tA, tB);
      put_unit(rs, W_O4 + (unsigned)jb * 10240 + (unsigned)(320 + tid) * 16, __float_as_uint(a10), __float_as_uint(a11), tA, tB);
    }
  }
  STAMP(8);
  if (st[PROBE] != 0) {
    const u64 target = st[0] + 10000ull + 8ull * (st[PROBE] - st[0]);
    for (int sp = 0; sp < 200000; ++sp) { if (__builtin_amdgcn_s_memrealtime() >= target) break; __builtin_amdgcn_s_sleep(4); }
  }
}

extern "C" void kernel_launch(void* const* d_in, const int* in_sizes, int n_in, void* d_out, int out_size, void* d_ws, size_t ws_size, hipStream_t stream) {
  fused_kernel<<<256, NTHR, 0, stream>>>((const u16*)d_in[0], (const u16*)d_in[1], (const u16*)d_in[2], (const u16*)d_in[3], (const u16*)d_in[4], (const u16*)d_in[6],
                                         (const u16*)d_in[7], (const u16*)d_in[8], (const u16*)d_in[9], (u16*)d_out, (unsigned char*)d_ws);
}
```

```cpp
#include <hip/hip_runtime.h>
#include <hip/hip_bf16.h>
#include <stdint.h>

typedef unsigned u32x4 __attribute__((ext_vector_type(4)));
typedef unsigned u32x2 __attribute__((ext_vector_type(2)));
typedef float f32x4 __attribute__((ext_vector_type(4)));
typedef float f32x2 __attribute__((ext_vector_type(2)));
typedef __bf16 bf16x2 __attribute__((ext_vector_type(2)));
typedef unsigned short u16;
typedef unsigned long long u64;

#define STAMPS 0
#define PD 3

#define NSLOT 3
#define NS0 256
#define NS1 256
#define THR 40
#define XSTEP 0
#define LATE 1
constexpr int DM = 640, NH = 32, SK = 8192, DH = 192, FF = 1536;
constexpr int NW = 8;
constexpr int NTHR = NW * 64;
constexpr int NRED = 80, NMLP = 64;
constexpr int RINGW = 18432;
constexpr int L_RING = 0;
constexpr int L_QP = NW * RINGW;
constexpr int L_Q = L_QP + 1536;
constexpr int L_MISC = L_Q + 384;
constexpr int L_T1 = L_MISC + 64;
constexpr int L_T2 = L_T1 + 8192;
constexpr int L_CS = L_T2 + 2560;
constexpr int L_ST = L_CS + 768;
constexpr int L_END = L_ST + 128;
static_assert(L_END <= 163840, "lds");
constexpr unsigned W_EPOCH = 0;
constexpr unsigned W_QX = 256;
constexpr unsigned W_DN = 53248;
constexpr unsigned W_P1 = 65536;
constexpr unsigned W_Y2 = W_P1 + 256 * 1792;
constexpr unsigned W_H3 = W_Y2 + 256 * 1280;
constexpr unsigned W_O4 = W_H3 + 5120;
constexpr unsigned W_END = W_O4 + 64 * 10240;
constexpr unsigned W_STAMP = 4u << 20;
constexpr unsigned SPIN_CAP = 400000;
constexpr int NSTAMP = 10;

__device__ __forceinline__ float rbf(float x) { return (float)(__bf16)x; }
__device__ __forceinline__ float bflo(unsigned v) { return __builtin_bit_cast(float, v << 16); }
__device__ __forceinline__ float bfhi(unsigned v) { return __builtin_bit_cast(float, v & 0xffff0000u); }
__device__ __forceinline__ float bf1(unsigned v) { return __builtin_bit_cast(float, v << 16); }
__device__ __forceinline__ unsigned pack2(float lo, float hi) { bf16x2 v = {(__bf16)lo, (__bf16)hi}; return __builtin_bit_cast(unsigned, v); }
__device__ __forceinline__ float dot2(unsigned k, unsigned q, float acc) {
  return __builtin_amdgcn_fdot2_f32_bf16(__builtin_bit_cast(bf16x2, k), __builtin_bit_cast(bf16x2, q), acc, false);
}
__device__ __forceinline__ unsigned plo(unsigned r0, unsigned r1) { return __builtin_amdgcn_perm(r1, r0, 0x05040100u); }
__device__ __forceinline__ unsigned phi(unsigned r0, unsigned r1) { return __builtin_amdgcn_perm(r1, r0, 0x07060302u); }
template <int CTRL> __device__ __forceinline__ float dpp(float x) {
  return __builtin_bit_cast(float, __builtin_amdgcn_mov_dpp(__builtin_bit_cast(int, x), CTRL, 0xf, 0xf, true));
}
constexpr int XOR1 = 0xB1, XOR2 = 0x4E, HMIR = 0x141, MIR = 0x140, ROR8 = 0x128, ROR4 = 0x124;
__device__ __forceinline__ float sw32(float a, float b) {
  auto s = __builtin_amdgcn_permlane32_swap(__float_as_uint(a), __float_as_uint(b), false, false);
  return __uint_as_float(s[0]) + __uint_as_float(s[1]);
}
__device__ __forceinline__ float sw16(float a, float b) {
  auto s = __builtin_amdgcn_permlane16_swap(__float_as_uint(a), __float_as_uint(b), false, false);
  return __uint_as_float(s[0]) + __uint_as_float(s[1]);
}
__device__ __forceinline__ float xrow_max(float x) {
  auto s = __builtin_amdgcn_permlane16_swap(__float_as_uint(x), __float_as_uint(x), false, false);
  x = fmaxf(__uint_as_float(s[0]), __uint_as_float(s[1]));
  auto t = __builtin_amdgcn_permlane32_swap(__float_as_uint(x), __float_as_uint(x), false, false);
  return fmaxf(__uint_as_float(t[0]), __uint_as_float(t[1]));
}
__device__ __forceinline__ float grp8_sum(float x) { x += dpp<XOR1>(x); x += dpp<XOR2>(x); x += dpp<HMIR>(x); return x; }
__device__ __forceinline__ void lds_barrier() { asm volatile("s_waitcnt lgkmcnt(0)" ::: "memory"); __builtin_amdgcn_s_barrier(); asm volatile("" ::: "memory"); }
__device__ __forceinline__ float rdlane(float x, int l) { return __builtin_bit_cast(float, __builtin_amdgcn_readlane(__builtin_bit_cast(int, x), l)); }

__device__ __forceinline__ f32x2 wave_sum2(float a, float b) {
  float t = sw32(a, b);
  t = sw16(t, t);
  t = grp8_sum(t);
  t += dpp<MIR>(t);
  return f32x2{rdlane(t, 0), rdlane(t, 32)};
}
__device__ __forceinline__ void ln_stats2(const unsigned (&r)[2][5], float (&mb)[2], float (&rd)[2]) {
  float s[2] = {0.f, 0.f};
#pragma unroll
  for (int i = 0; i < 5; ++i) { s[0] = dot2(r[0][i], 0x3f803f80u, s[0]); s[1] = dot2(r[1][i], 0x3f803f80u, s[1]); }
  const f32x2 mean = wave_sum2(s[0], s[1]) * (1.0f / DM);
  f32x2 q[2] = {{0.f, 0.f}, {0.f, 0.f}};
#pragma unroll
  for (int i = 0; i < 5; ++i)
#pragma unroll
    for (int bb = 0; bb < 2; ++bb) { const f32x2 d = f32x2{bflo(r[bb][i]), bfhi(r[bb][i])} - mean[bb]; q[bb] = __builtin_elementwise_fma(d, d, q[bb]); }
  const f32x2 var = wave_sum2(q[0][0] + q[0][1], q[1][0] + q[1][1]) * (1.0f / DM);
#pragma unroll
  for (int bb = 0; bb < 2; ++bb) {
    mb[bb] = rbf(mean[bb]);
    rd[bb] = __builtin_amdgcn_rcpf(rbf(__builtin_amdgcn_sqrtf(rbf(rbf(var[bb]) + 1e-5f))));
  }
}
__device__ __forceinline__ unsigned ln_pair(unsigned v, float mb, float rd) {
  const unsigned d = pack2(bflo(v) - mb, bfhi(v) - mb);
  return pack2(bflo(d) * rd, bfhi(d) * rd);
}
__device__ __forceinline__ unsigned ln_one(unsigned v, float mb, float rd) { return pack2(rbf(bf1(v) - mb) * rd, 0.f); }

__device__ __forceinline__ void dma_half(unsigned lds, const char* gb, unsigned voff) {
  unsigned keep;
  asm volatile(
      "s_mov_b32 %0, m0\n\t"
      "s_mov_b32 m0, %3\n\t"
      "s_nop 0\n\t"
      "global_load_lds_dwordx4 %1, %2 nt\n\t"
      "s_add_u32 m0, m0, 0x380\n\t"
      "s_nop 0\n\t"
      "global_load_lds_dwordx4 %1, %2 offset:128 nt\n\t"
      "s_add_u32 m0, m0, 0x380\n\t"
      "s_nop 0\n\t"
      "global_load_lds_dwordx4 %1, %2 offset:256 nt\n\t"
      "s_mov_b32 m0, %0"
      : "=&s"(keep) : "v"(voff), "s"(gb), "s"(lds) : "memory", "scc");
}
__device__ __forceinline__ void dma_step(unsigned lds, const char* kb, const char* vb, unsigned voff) {
  unsigned keep;
  asm volatile(
      "s_waitcnt lgkmcnt(0)\n\t"
      "s_mov_b32 %0, m0\n\t"
      "s_mov_b32 m0, %4\n\t"
      "s_nop 0\n\t"
      "global_load_lds_dwordx4 %1, %2 nt\n\t"
      "s_add_u32 m0, m0, 0x380\n\t"
      "s_nop 0\n\t"
      "global_load_lds_dwordx4 %1, %2 offset:128 nt\n\t"
      "s_add_u32 m0, m0, 0x380\n\t"
      "s_nop 0\n\t"
      "global_load_lds_dwordx4 %1, %2 offset:256 nt\n\t"
      "s_add_u32 m0, m0, 0x500\n\t"
      "s_nop 0\n\t"
      "global_load_lds_dwordx4 %1, %3 nt\n\t"
      "s_add_u32 m0, m0, 0x380\n\t"
      "s_nop 0\n\t"
      "global_load_lds_dwordx4 %1, %3 offset:128 nt\n\t"
      "s_add_u32 m0, m0, 0x380\n\t"
      "s_nop 0\n\t"
      "global_load_lds_dwordx4 %1, %3 offset:256 nt\n\t"
      "s_mov_b32 m0, %0"
      : "=&s"(keep) : "v"(voff), "s"(kb), "s"(vb), "s"(lds) : "memory", "scc");
}
#define LD4NT(dst, voff, base, off) asm volatile("global_load_dwordx4 %0, %1, %2 offset:%c3 nt" : "=v"(dst) : "v"(voff), "s"(base), "i"(off) : "memory")
#define LD4NTP(dst, voff, base, off) asm volatile("global_load_dwordx4 %0, %1, %2 offset:%c3 nt" : "+v"(dst) : "v"(voff), "s"(base), "i"(off) : "memory")
#define LD1NT(dst, voff, base, off) asm volatile("global_load_dword %0, %1, %2 offset:%c3 nt" : "=v"(dst) : "v"(voff), "s"(base), "i"(off) : "memory")
#define LD4(dst, voff, base, off) asm volatile("global_load_dwordx4 %0, %1, %2 offset:%c3" : "=v"(dst) : "v"(voff), "s"(base), "i"(off) : "memory")
#define LD2(dst, voff, base, off) asm volatile("global_load_dwordx2 %0, %1, %2 offset:%c3" : "=v"(dst) : "v"(voff), "s"(base), "i"(off) : "memory")
#define LD1(dst, voff, base, off) asm volatile("global_load_dword %0, %1, %2 offset:%c3" : "=v"(dst) : "v"(voff), "s"(base), "i"(off) : "memory")
#define LDU16(dst, voff, base, off) asm volatile("global_load_ushort %0, %1, %2 offset:%c3" : "=v"(dst) : "v"(voff), "s"(base), "i"(off) : "memory")
#define WAITVM(n) asm volatile("s_waitcnt vmcnt(" #n ")" ::: "memory")
#define WAITVMX(n) WAITVM(n)

__device__ __forceinline__ void put_unit(__amdgpu_buffer_rsrc_t rs, unsigned off, unsigned v0, unsigned v1, unsigned tA, unsigned tB) {
  u32x4 u = {tA ^ v1, v0, tB ^ v0, v1};
  __builtin_amdgcn_raw_buffer_store_b128(u, rs, off, 0, 16);
}
__device__ __forceinline__ bool unit_ok(const u32x4& v, unsigned tA, unsigned tB) { return ((v[0] ^ v[3]) == tA) && ((v[2] ^ v[1]) == tB); }
template <int N>
__device__ __forceinline__ void get_units(__amdgpu_buffer_rsrc_t rs, const unsigned (&off)[N], u32x4 (&v)[N], bool active, unsigned tA, unsigned tB) {
  bool ok = !active;
#pragma nounroll
  for (unsigned spin = 0; spin < SPIN_CAP; ++spin) {
    if (!ok) {
      asm volatile("" ::: "memory");
#pragma unroll
      for (int k = 0; k < N; ++k) v[k] = __builtin_amdgcn_raw_buffer_load_b128(rs, off[k], 0, 16);
      bool g = true;
#pragma unroll
      for (int k = 0; k < N; ++k) g = g && unit_ok(v[k], tA, tB);
      ok = g;
    }
    if (__all(ok)) break;
    __builtin_amdgcn_s_sleep(1);
  }
}
__device__ __forceinline__ f32x4 red16_g4(const float (&a)[16]) {
  float t[8], u[4];
#pragma unroll
  for (int k = 0; k < 8; ++k) t[k] = sw32(a[k], a[8 + k]);
#pragma unroll
  for (int k = 0; k < 4; ++k) { u[k] = sw16(t[k], t[k + 4]); u[k] += dpp<ROR8>(u[k]); u[k] += dpp<ROR4>(u[k]); }
  return f32x4{u[0], u[1], u[2], u[3]};
}

__global__ __launch_bounds__(NTHR) void fused_kernel(const u16* __restrict__ x, const u16* __restrict__ Kc, const u16* __restrict__ Vc, const u16* __restrict__ wq,
                                                      const u16* __restrict__ wo, const u16* __restrict__ w1, const u16* __restrict__ b1, const u16* __restrict__ w2,
                                                      const u16* __restrict__ b2, u16* __restrict__ out, unsigned char* __restrict__ ws) {
  __shared__ __attribute__((aligned(16))) unsigned char lds[L_END];
  const int bid = blockIdx.x, loc = bid >> 3;
  const int h = (bid & 7) * 4 + (loc >> 3), sx = loc & 7, b = sx >> 2, j = sx & 3, g = b * NH + h;
  const int tid = threadIdx.x, lane = tid & 63, wave = __builtin_amdgcn_readfirstlane(tid >> 6), r = lane >> 3, c = lane & 7;
  const unsigned lds0 = __builtin_amdgcn_readfirstlane((unsigned)(uintptr_t)(__attribute__((address_space(3))) unsigned char*)lds);
  const __amdgpu_buffer_rsrc_t rs = __builtin_amdgcn_make_buffer_rsrc(ws, 0, W_END, 0x00020000);
  float* qp = reinterpret_cast<float*>(lds + L_QP);
  unsigned* ql = reinterpret_cast<unsigned*>(lds + L_Q);
  unsigned* misc = reinterpret_cast<unsigned*>(lds + L_MISC);
  float* t1 = reinterpret_cast<float*>(lds + L_T1);
  unsigned* t2 = reinterpret_cast<unsigned*>(lds + L_T2);
#define STAMP(i)
  STAMP(0);
  const bool is_mlp = j == 0, is_op = j != 0 && sx != 7, is_red = sx == 7 && h + 32 * wave < NRED;
  const int jb = g, oi = sx - 1 - b, rid = h + 32 * wave;
  const int s0 = j == 0 ? 0 : NS0 + (j - 1) * NS1, nstep = j == 0 ? NS0 : (j == 3 ? 1024 - NS0 - 2 * NS1 : NS1);
  const char* kblk = reinterpret_cast<const char*>(Kc) + ((size_t)g * SK + (size_t)s0 * 8) * 384;
  const char* vblk = reinterpret_cast<const char*>(Vc) + ((size_t)g * SK + (size_t)s0 * 8) * 384;
  const unsigned ring = lds0 + L_RING + wave * RINGW;
  const unsigned kvoff = r * 384 + c * 16;
  const int g4 = lane >> 2, c4 = lane & 3, rg = lane >> 4, c16 = lane & 15;
  u32x2 ep;
  if (wave == NW - 1) {
    asm volatile("global_load_dwordx2 %0, %1, %2 sc1" : "=v"(ep) : "v"(0u), "s"(ws) : "memory");
    if (lane == 0) { misc[5] = 0; misc[6] = 0; }
  }
  {
    u32x4 xa[2], wv[5];
    unsigned xb[2], xs[2];
    u32x2 xo[2];
    const unsigned o16 = lane * 16, o4 = lane * 4, oo = (80 * wave + 4 * g4) * 2, os = (80 * wave + 64 + g4) * 2;
    LD4(xa[0], o16, x, 0); LD1(xb[0], o4, x, 1024); LD4(xa[1], o16, x, 1280); LD1(xb[1], o4, x, 2304);
    LD2(xo[0], oo, x, 0); LDU16(xs[0], os, x, 0); LD2(xo[1], oo, x, 1280); LDU16(xs[1], os, x, 1280);
    const unsigned wcol = (unsigned)(h * 384 + sx * 48 + (c4 < 3 ? c4 : 2) * 16);
    const unsigned wa = (unsigned)(80 * wave + 4 * g4) * 12288u + wcol, wb = (unsigned)(80 * wave + 64 + g4) * 12288u + wcol;
    const char* wqc = reinterpret_cast<const char*>(wq);
    LD4NT(wv[0], wa, wqc, 0); LD4NT(wv[1], wa, wqc + 12288, 0); LD4NT(wv[2], wa, wqc + 24576, 0); LD4NT(wv[3], wa, wqc + 36864, 0); LD4NT(wv[4], wb, wqc, 0);
    dma_half(ring, kblk + (size_t)wave * 3072, kvoff);
    dma_half(ring + 3072, vblk + (size_t)wave * 3072, kvoff);
    asm volatile("s_waitcnt vmcnt(11)" : "+v"(xa[0]), "+v"(xa[1]), "+v"(xb[0]), "+v"(xb[1]), "+v"(xo[0]), "+v"(xo[1]), "+v"(xs[0]), "+v"(xs[1])::"memory");
    const unsigned xr[2][5] = {{xa[0][0], xa[0][1], xa[0][2], xa[0][3], xb[0]}, {xa[1][0], xa[1][1], xa[1][2], xa[1][3], xb[1]}};
    float mb[2], rd[2];
    ln_stats2(xr, mb, rd);
    unsigned xp[2][3];
#pragma unroll
    for (int bb = 0; bb < 2; ++bb) { xp[bb][0] = ln_pair(xo[bb][0], mb[bb], rd[bb]); xp[bb][1] = ln_pair(xo[bb][1], mb[bb], rd[bb]); xp[bb][2] = ln_one(xs[bb], mb[bb], rd[bb]); }
    asm volatile("s_waitcnt vmcnt(6)" : "+v"(wv[0]), "+v"(wv[1]), "+v"(wv[2]), "+v"(wv[3]), "+v"(wv[4]), "+v"(xp[0][0]), "+v"(xp[0][1]), "+v"(xp[0][2]), "+v"(xp[1][0]), "+v"(xp[1][1]), "+v"(xp[1][2])::"memory");
    STAMP(1);
    float a[16];
#pragma unroll
    for (int k = 0; k < 4; ++k) {
      const unsigned p0 = plo(wv[0][k], wv[1][k]), p1 = phi(wv[0][k], wv[1][k]), p2 = plo(wv[2][k], wv[3][k]), p3 = phi(wv[2][k], wv[3][k]);
      const unsigned p4 = wv[4][k] & 0xffffu, p5 = wv[4][k] >> 16;
#pragma unroll
      for (int bb = 0; bb < 2; ++bb) {
        a[bb * 8 + 2 * k] = dot2(p4, xp[bb][2], dot2(p2, xp[bb][1], dot2(p0, xp[bb][0], 0.f)));
        a[bb * 8 + 2 * k + 1] = dot2(p5, xp[bb][2], dot2(p3, xp[bb][1], dot2(p1, xp[bb][0], 0.f)));
      }
    }
    const f32x4 s4 = red16_g4(a);
    if ((lane & 12) == 0 && c4 < 3) *reinterpret_cast<f32x4*>(qp + wave * 48 + (lane >> 5) * 24 + c4 * 8 + ((lane >> 4) & 1) * 4) = s4;
  }
  lds_barrier();
  STAMP(2);
  if (wave == NW - 1) {
    asm volatile("" : "+v"(ep));
    const u64 e1 = (((u64)ep[1] << 32) | ep[0]) + 1ull;
    const unsigned tA = (unsigned)e1, tB = (((unsigned)e1 * 0x9E3779B1u) ^ (unsigned)(e1 >> 32)) | 1u;
    if (lane < 12) {
      f32x4 a = {0.f, 0.f, 0.f, 0.f};
#pragma unroll
      for (int w = 0; w < NW; ++w) a += *reinterpret_cast<const f32x4*>(qp + w * 48 + lane * 4);
      put_unit(rs, W_QX + (unsigned)((h * 8 + sx) * 12 + lane) * 16, pack2(a[0], a[1]), pack2(a[2], a[3]), tA, tB);
    }
    if (lane == 0) { misc[0] = (NSLOT + 3 + XSTEP) * NW; misc[1] = tA; misc[2] = tB; misc[3] = (unsigned)e1; misc[4] = (unsigned)(e1 >> 32); }
    const int li = lane < 48 ? lane : 0;
    unsigned off[1] = {W_QX + (unsigned)((h * 8 + li / 6) * 12 + b * 6 + li % 6) * 16};
    u32x4 v[1];
    get_units<1>(rs, off, v, lane < 48, tA, tB);
    if (lane < 48) *reinterpret_cast<u32x2*>(ql + 2 * lane) = u32x2{v[0][1], v[0][3]};
    asm volatile("s_waitcnt lgkmcnt(0)" ::: "memory");
    if (lane == 0) __hip_atomic_store(misc + 5, 1u, __ATOMIC_RELAXED, __HIP_MEMORY_SCOPE_WORKGROUP);
    STAMP(9);
  }
  u32x4 wov[6];
  unsigned bsm = 0, rx = 0, rb2 = 0;
  const char* w1c = reinterpret_cast<const char*>(w1) + jb * 48;
  const char* w2c = reinterpret_cast<const char*>(w2) + (size_t)jb * 30720;
  {
    if (is_mlp) {
      const unsigned bo = (unsigned)(jb * 24 + (lane < 48 ? lane % 24 : 0)) * 2;
      LDU16(bsm, bo, b1, 0);
      LD1(rx, (unsigned)(lane & 1) * (639u * 3072u), w1c, 0);
      LD1(rb2, (unsigned)(lane & 1) * 30716u, w2c, 0);
    } else if (is_red) {
      const unsigned col = (unsigned)((rid % 40) * 16 + 2 * c + (lane >> 5));
      LDU16(rx, col * 2, x + (rid / 40) * DM, 0);
      LDU16(rb2, col * 2, b2, 0);
    }
    WAITVMX(PD); dma_half(ring + 6144, kblk + (size_t)(wave + 8) * 3072, kvoff);
    WAITVMX(PD); dma_half(ring + 9216, vblk + (size_t)(wave + 8) * 3072, kvoff);
    WAITVMX(PD); dma_half(ring + 12288, kblk + (size_t)(wave + 16) * 3072, kvoff);
    WAITVMX(PD); dma_half(ring + 15360, vblk + (size_t)(wave + 16) * 3072, kvoff);
    if (is_op) {
      const char* woc = reinterpret_cast<const char*>(wo) + (size_t)(h * DH + 24 * wave) * 1280 + oi * 256;
      const unsigned oa = (unsigned)(4 * rg) * 1280u + c16 * 16, ob = (unsigned)(16 + 2 * rg) * 1280u + c16 * 16;
      WAITVMX(PD); LD4NT(wov[0], oa, woc, 0); LD4NT(wov[1], oa, woc, 1280); LD4NT(wov[2], oa, woc, 2560);
      WAITVMX(PD); LD4NT(wov[3], oa, woc, 3840); LD4NT(wov[4], ob, woc, 0); LD4NT(wov[5], ob, woc, 1280);
    }
  }
  while (__hip_atomic_load(misc + 5, __ATOMIC_RELAXED, __HIP_MEMORY_SCOPE_WORKGROUP) == 0) __builtin_amdgcn_s_sleep(1);
  asm volatile("" ::: "memory");
  STAMP(3);
  const unsigned tA = misc[1], tB = misc[2];
  float m = -INFINITY, l = 0.f;
  float acc[24];
  unsigned wop[24];
  {
    u32x4 q[3];
#pragma unroll
    for (int t = 0; t < 3; ++t) q[t] = *reinterpret_cast<const u32x4*>(ql + t * 32 + c * 4);
#pragma unroll
    for (int k = 0; k < 24; ++k) acc[k] = 0.f;
    constexpr float SC = 0.07216878364870322f, L2E = 1.4426950408889634f;
    int nxt = (NSLOT + XSTEP) * NW + wave;
    int slot = 0, it = 0;
    const unsigned char* rp = lds + L_RING + wave * RINGW + lane * 16;
    auto step = [&](const u32x4 (&kb)[3], const u32x4 (&vb)[3]) {
      float d = 0.f;
#pragma unroll
      for (int t = 0; t < 3; ++t)
#pragma unroll
        for (int k = 0; k < 4; ++k) d = dot2(kb[t][k], q[t][k], d);
      d = grp8_sum(d);
      const float s_ = rbf(rbf(d) * SC) * L2E;
      if (__any(s_ > m)) {
        const float mx = fmaxf(m, s_), al = __builtin_amdgcn_exp2f(m - mx);
        l *= al;
#pragma unroll
        for (int k = 0; k < 24; ++k) acc[k] *= al;
        m = mx;
      }
      const float p = __builtin_amdgcn_exp2f(s_ - m);
      l += p;
#pragma unroll
      for (int t = 0; t < 3; ++t)
#pragma unroll
        for (int k = 0; k < 4; ++k) {
          acc[t * 8 + 2 * k] = fmaf(p, bflo(vb[t][k]), acc[t * 8 + 2 * k]);
          acc[t * 8 + 2 * k + 1] = fmaf(p, bfhi(vb[t][k]), acc[t * 8 + 2 * k + 1]);
        }
    };
#pragma nounroll
    for (; nxt < nstep; ++it) {
      asm volatile("s_waitcnt vmcnt(12)" ::: "memory");
      u32x4 kb[3], vb[3];
#pragma unroll
      for (int t = 0; t < 3; ++t) { kb[t] = *reinterpret_cast<const u32x4*>(rp + slot * 6144 + t * 1024); vb[t] = *reinterpret_cast<const u32x4*>(rp + slot * 6144 + 3072 + t * 1024); }
      dma_step(ring + slot * 6144, kblk + (size_t)nxt * 3072, vblk + (size_t)nxt * 3072, kvoff);
      if (it < 2) nxt += NW;
      else {
        int n2 = 0;
        if (lane == 0) n2 = __hip_atomic_fetch_add(misc, 1u, __ATOMIC_RELAXED, __HIP_MEMORY_SCOPE_WORKGROUP);
        nxt = __builtin_amdgcn_readfirstlane(n2);
      }
      step(kb, vb);
      if (it == 2) {
        asm volatile("s_waitcnt vmcnt(18) ; X_LANDED" : "+v"(bsm), "+v"(rx), "+v"(rb2), "+v"(wov[0]), "+v"(wov[1]), "+v"(wov[2]), "+v"(wov[3]), "+v"(wov[4]), "+v"(wov[5])::"memory");
#pragma unroll
        for (int pr = 0; pr < 3; ++pr)
#pragma unroll
          for (int k = 0; k < 4; ++k) { wop[pr * 8 + 2 * k] = plo(wov[2 * pr][k], wov[2 * pr + 1][k]); wop[pr * 8 + 2 * k + 1] = phi(wov[2 * pr][k], wov[2 * pr + 1][k]); }
      }
      slot = slot == NSLOT - 1 ? 0 : slot + 1;
    }
#pragma nounroll
    for (int wc = 2; wc >= 0; --wc) {
      if (wc == 2) asm volatile("s_waitcnt vmcnt(12)" ::: "memory");
      else if (wc == 1) asm volatile("s_waitcnt vmcnt(6)" ::: "memory");
      else asm volatile("s_waitcnt vmcnt(0)" ::: "memory");
      u32x4 kb[3], vb[3];
#pragma unroll
      for (int t = 0; t < 3; ++t) { kb[t] = *reinterpret_cast<const u32x4*>(rp + slot * 6144 + t * 1024); vb[t] = *reinterpret_cast<const u32x4*>(rp + slot * 6144 + 3072 + t * 1024); }
      step(kb, vb);
      slot = slot == NSLOT - 1 ? 0 : slot + 1;
    }
    STAMP(4);
    const float M = xrow_max(fmaxf(m, dpp<ROR8>(m)));
    const float al = __builtin_amdgcn_exp2f(m - M);
    l *= al; l += dpp<ROR8>(l); l = sw16(l, l); l = sw32(l, l);
    float t12[12], t6[6];
#pragma unroll
    for (int k = 0; k < 12; ++k) t12[k] = sw32(acc[k] * al, acc[k + 12] * al);
#pragma unroll
    for (int k = 0; k < 6; ++k) { t6[k] = sw16(t12[k], t12[k + 6]); t6[k] += dpp<ROR8>(t6[k]); }
    float* rec = reinterpret_cast<float*>(lds + L_RING + wave * RINGW);
    if ((lane & 8) == 0) {
      float* p = rec + 4 + c * 24 + 12 * (lane >> 5) + 6 * ((lane >> 4) & 1);
#pragma unroll
      for (int k = 0; k < 3; ++k) *reinterpret_cast<f32x2*>(p + 2 * k) = f32x2{t6[2 * k], t6[2 * k + 1]};
      if (lane == 0) *reinterpret_cast<f32x2*>(rec) = f32x2{M, l};
    }
  }
  lds_barrier();
  if (tid < 97) {
    const int d = 2 * tid, idx = tid < 96 ? 4 + ((d & 63) >> 3) * 24 + (d >> 6) * 8 + (d & 7) : 0;
    f32x2 ml[NW], a[NW];
#pragma unroll
    for (int w = 0; w < NW; ++w) {
      const float* rec = reinterpret_cast<const float*>(lds + L_RING + w * RINGW);
      ml[w] = *reinterpret_cast<const f32x2*>(rec); a[w] = *reinterpret_cast<const f32x2*>(rec + idx);
    }
    float M = ml[0][0];
#pragma unroll
    for (int w = 1; w < NW; ++w) M = fmaxf(M, ml[w][0]);
    f32x2 o = {0.f, 0.f};
#pragma unroll
    for (int w = 0; w < NW; ++w) { const float wt = __builtin_amdgcn_exp2f(ml[w][0] - M); o = __builtin_elementwise_fma(f32x2{wt, wt}, a[w], o); }
    put_unit(rs, W_P1 + (unsigned)(h * 8 + sx) * 1792 + tid * 16, __float_as_uint(tid < 96 ? o[0] : M), __float_as_uint(o[1]), tA, tB);
  }
  if (tid == 128) put_unit(rs, W_DN + (unsigned)bid * 16, 0u, 0u, tA, tB);
  if (is_op) {
  if (wave >= 5) {
    const int t3 = tid - 320, bb = t3 >= 96, u = t3 - 96 * bb;
    const unsigned base = W_P1 + (unsigned)(h * 8 + bb * 4) * 1792;
    u32x4 v[4], f[4];
#pragma unroll
    for (int jj = 0; jj < 4; ++jj) { f[jj] = __builtin_amdgcn_raw_buffer_load_b128(rs, base + jj * 1792 + 96 * 16, 0, 16); v[jj] = __builtin_amdgcn_raw_buffer_load_b128(rs, base + jj * 1792 + u * 16, 0, 16); }
#pragma nounroll
    for (unsigned spin = 0; spin < SPIN_CAP; ++spin) {
      bool okf[4], okv[4];
      int miss = 0;
      bool all = true;
#pragma unroll
      for (int jj = 0; jj < 4; ++jj) { okf[jj] = unit_ok(f[jj], tA, tB); okv[jj] = unit_ok(v[jj], tA, tB); miss += okf[jj] ? 0 : 1; all = all && okf[jj] && okv[jj]; }
      if (__all(all)) break;
      __builtin_amdgcn_s_sleep(1);
      asm volatile("" ::: "memory");
#pragma unroll
      for (int jj = 0; jj < 4; ++jj) {
        if (!okf[jj]) f[jj] = __builtin_amdgcn_raw_buffer_load_b128(rs, base + jj * 1792 + 96 * 16, 0, 16);
        if (!okv[jj] && (okf[jj] || miss <= 1)) v[jj] = __builtin_amdgcn_raw_buffer_load_b128(rs, base + jj * 1792 + u * 16, 0, 16);
      }
    }
    const float M = fmaxf(fmaxf(__uint_as_float(f[0][1]), __uint_as_float(f[1][1])), fmaxf(__uint_as_float(f[2][1]), __uint_as_float(f[3][1])));
    float L = 0.f, o0 = 0.f, o1 = 0.f;
#pragma unroll
    for (int jj = 0; jj < 4; ++jj) {
      const float wt = __builtin_amdgcn_exp2f(__uint_as_float(f[jj][1]) - M);
      L = fmaf(wt, __uint_as_float(f[jj][3]), L); o0 = fmaf(wt, __uint_as_float(v[jj][1]), o0); o1 = fmaf(wt, __uint_as_float(v[jj][3]), o1);
    }
    const float rl = __builtin_amdgcn_rcpf(L);
    t2[t3] = pack2(o0 * rl, o1 * rl);
  }
  lds_barrier();
  STAMP(5);
  {
    float a[16];
#pragma unroll
    for (int bb = 0; bb < 2; ++bb) {
      const u32x2 p01 = *reinterpret_cast<const u32x2*>(t2 + bb * 96 + 12 * wave + 2 * rg);
      const unsigned p2 = t2[bb * 96 + 12 * wave + 8 + rg];
#pragma unroll
      for (int k = 0; k < 8; ++k) a[bb * 8 + k] = dot2(wop[16 + k], p2, dot2(wop[8 + k], p01[1], dot2(wop[k], p01[0], 0.f)));
    }
    float t[8];
#pragma unroll
    for (int k = 0; k < 8; ++k) t[k] = sw32(a[k], a[8 + k]);
    const f32x4 s4 = {sw16(t[0], t[4]), sw16(t[1], t[5]), sw16(t[2], t[6]), sw16(t[3], t[7])};
    *reinterpret_cast<f32x4*>(t1 + wave * 256 + (lane >> 5) * 128 + c16 * 8 + ((lane >> 4) & 1) * 4) = s4;
  }
  lds_barrier();
  if (tid < 128) {
    f32x2 a = {0.f, 0.f};
#pragma unroll
    for (int w = 0; w < NW; ++w) a += *reinterpret_cast<const f32x2*>(t1 + w * 256 + 2 * tid);
    put_unit(rs, W_Y2 + (unsigned)(h * 5 + oi) * 2048 + tid * 16, __float_as_uint(a[0]), __float_as_uint(a[1]), tA, tB);
  }
  STAMP(6);
  } else if (!is_mlp) {
    if (is_red) {
      const int bq = rid / 40, cg = rid % 40;
      float hv;
      {
        unsigned off[4];
#pragma unroll
        for (int k = 0; k < 4; ++k) off[k] = W_Y2 + (unsigned)((4 * r + k) * 5 + (cg >> 3)) * 2048 + (unsigned)(bq * 64 + (cg & 7) * 8 + c) * 16;
        u32x4 v[4];
        get_units<4>(rs, off, v, true, tA, tB);
        const float a0 = (__uint_as_float(v[0][1]) + __uint_as_float(v[1][1])) + (__uint_as_float(v[2][1]) + __uint_as_float(v[3][1]));
        const float a1 = (__uint_as_float(v[0][3]) + __uint_as_float(v[1][3])) + (__uint_as_float(v[2][3]) + __uint_as_float(v[3][3]));
        float s = sw32(a0, a1); s = sw16(s, s); s += dpp<ROR8>(s);
        hv = rbf(bf1(rx) + rbf(s));
        auto e = __builtin_amdgcn_permlane32_swap(__float_as_uint(hv), __float_as_uint(hv), false, false);
        const unsigned pk = (e[0] >> 16) | (e[1] & 0xffff0000u);
        const unsigned pn = (unsigned)__builtin_amdgcn_mov_dpp((int)pk, XOR1, 0xf, 0xf, true);
        if (lane < 8 && !(lane & 1)) put_unit(rs, W_H3 + (unsigned)(rid * 4 + (lane >> 1)) * 16, pk, pn, tA, tB);
      }
      STAMP(7);
      {
        unsigned off[8];
#pragma unroll
        for (int k = 0; k < 8; ++k) off[k] = W_O4 + (unsigned)(8 * r + k) * 10240 + (unsigned)(bq * 320 + cg * 8 + c) * 16;
        u32x4 v[8];
        get_units<8>(rs, off, v, true, tA, tB);
        float a0 = 0.f, a1 = 0.f;
#pragma unroll
        for (int k = 0; k < 8; ++k) { a0 += __uint_as_float(v[k][1]); a1 += __uint_as_float(v[k][3]); }
        float s = sw32(a0, a1); s = sw16(s, s); s += dpp<ROR8>(s);
        const float ov = rbf(hv + rbf(rbf(s) + bf1(rb2)));
        auto e = __builtin_amdgcn_permlane32_swap(__float_as_uint(ov), __float_as_uint(ov), false, false);
        if (lane < 8) reinterpret_cast<unsigned*>(out)[bq * 320 + cg * 8 + lane] = (e[0] >> 16) | (e[1] & 0xffff0000u);
        if (rid == 0 && lane == 0) __hip_atomic_store(reinterpret_cast<u64*>(ws + W_EPOCH), ((u64)misc[4] << 32) | misc[3], __ATOMIC_RELAXED, __HIP_MEMORY_SCOPE_AGENT);
      }
    }
  } else {
    const int li = lane < 48 ? lane : 0, lb = li >= 24;
    float csum = 0.f;
    unsigned w1p[24], w2v[24];
    {
      if (LATE) {
        if (wave == 7) {
          const unsigned smo = W_DN + (unsigned)((bid & 192) + lane) * 16;
#pragma nounroll
          for (unsigned spin = 0; spin < SPIN_CAP; ++spin) {
            const u32x4 v = __builtin_amdgcn_raw_buffer_load_b128(rs, smo, 0, 16);
            if (__builtin_popcountll(__ballot(unit_ok(v, tA, tB))) >= THR) break;
            __builtin_amdgcn_s_sleep(4);
          }
          if (lane == 0) __hip_atomic_store(misc + 6, 1u, __ATOMIC_RELAXED, __HIP_MEMORY_SCOPE_WORKGROUP);
        } else {
          while (__hip_atomic_load(misc + 6, __ATOMIC_RELAXED, __HIP_MEMORY_SCOPE_WORKGROUP) == 0) __builtin_amdgcn_s_sleep(1);
        }
      }
      u32x4 w1v[5];
      const unsigned w1a = (unsigned)(80 * wave + 4 * g4) * 3072u + (c4 < 3 ? c4 : 2) * 16, w1b = (unsigned)(80 * wave + 64 + g4) * 3072u + (c4 < 3 ? c4 : 2) * 16;
      LD4NT(w1v[0], w1a, w1c, 0); LD4NT(w1v[1], w1a, w1c, 3072); LD4NT(w1v[2], w1a, w1c + 6144, 0); LD4NT(w1v[3], w1a, w1c + 6144, 3072); LD4NT(w1v[4], w1b, w1c, 0);
      const unsigned o2 = (unsigned)(tid < 320 ? tid : 0) * 4;
      if (wave < 5) {
#pragma unroll
        for (int k = 0; k < 24; k += 3) { LD1NT(w2v[k], o2, w2c + k * 1280, 0); LD1NT(w2v[k + 1], o2, w2c + k * 1280, 1280); LD1NT(w2v[k + 2], o2, w2c + k * 1280, 2560); }
        asm volatile("s_waitcnt vmcnt(24)" ::: "memory");
      } else {
        asm volatile("s_waitcnt vmcnt(0)" ::: "memory");
      }
      asm volatile("; W1_LANDED" : "+v"(w1v[0]), "+v"(w1v[1]), "+v"(w1v[2]), "+v"(w1v[3]), "+v"(w1v[4])::"memory");
      STAMP(5);
#pragma unroll
      for (int k = 0; k < 4; ++k) {
        w1p[2 * k] = plo(w1v[0][k], w1v[1][k]); w1p[2 * k + 1] = phi(w1v[0][k], w1v[1][k]);
        w1p[8 + 2 * k] = plo(w1v[2][k], w1v[3][k]); w1p[8 + 2 * k + 1] = phi(w1v[2][k], w1v[3][k]);
        w1p[16 + 2 * k] = w1v[4][k] & 0xffffu; w1p[16 + 2 * k + 1] = w1v[4][k] >> 16;
      }
      float cs[8];
#pragma unroll
      for (int k = 0; k < 8; ++k) cs[k] = dot2(w1p[16 + k], 0x3f803f80u, dot2(w1p[8 + k], 0x3f803f80u, dot2(w1p[k], 0x3f803f80u, 0.f)));
      float c0 = sw16(sw32(cs[0], cs[4]), sw32(cs[2], cs[6])), c1 = sw16(sw32(cs[1], cs[5]), sw32(cs[3], cs[7]));
      c0 += dpp<ROR8>(c0); c0 += dpp<ROR4>(c0); c1 += dpp<ROR8>(c1); c1 += dpp<ROR4>(c1);
      if ((lane & 12) == 0 && c4 < 3) *reinterpret_cast<f32x2*>(lds + L_CS + (wave * 24 + c4 * 8 + 4 * (lane >> 5) + 2 * ((lane >> 4) & 1)) * 4) = f32x2{c0, c1};
      asm volatile("s_waitcnt vmcnt(0) ; W2_LANDED" : "+v"(w2v[0]), "+v"(w2v[1]), "+v"(w2v[2]), "+v"(w2v[3]), "+v"(w2v[4]), "+v"(w2v[5]), "+v"(w2v[6]), "+v"(w2v[7]), "+v"(w2v[8]), "+v"(w2v[9]), "+v"(w2v[10]), "+v"(w2v[11]),
                   "+v"(w2v[12]), "+v"(w2v[13]), "+v"(w2v[14]), "+v"(w2v[15]), "+v"(w2v[16]), "+v"(w2v[17]), "+v"(w2v[18]), "+v"(w2v[19]), "+v"(w2v[20]), "+v"(w2v[21]), "+v"(w2v[22]), "+v"(w2v[23]));
      STAMP(6);
#pragma unroll
      for (int i = 0; i < 12; ++i) { const unsigned lo = plo(w2v[2 * i], w2v[2 * i + 1]), hi = phi(w2v[2 * i], w2v[2 * i + 1]); w2v[2 * i] = lo; w2v[2 * i + 1] = hi; }
    }
    if (wave < 5) {
      unsigned off[1] = {W_H3 + (unsigned)tid * 16};
      u32x4 v[1];
      get_units<1>(rs, off, v, true, tA, tB);
      *reinterpret_cast<u32x2*>(t2 + 2 * tid) = u32x2{v[0][1], v[0][3]};
      float s = dot2(v[0][1], 0x3f803f80u, dot2(v[0][3], 0x3f803f80u, 0.f));
      s = grp8_sum(s); s += dpp<MIR>(s); s = sw16(s, s);
      const float mu = s * (1.0f / 128.f);
      const f32x2 d0 = f32x2{bflo(v[0][1]), bfhi(v[0][1])} - mu, d1 = f32x2{bflo(v[0][3]), bfhi(v[0][3])} - mu;
      const f32x2 q2 = __builtin_elementwise_fma(d0, d0, d1 * d1);
      float q = q2[0] + q2[1];
      q = grp8_sum(q); q += dpp<MIR>(q); q = sw16(q, q);
      if ((lane & 31) == 0) *reinterpret_cast<f32x2*>(lds + L_ST + (tid >> 5) * 8) = f32x2{mu, q};
    }
    lds_barrier();
    STAMP(7);
    {
      float a[16];
#pragma unroll
      for (int bb = 0; bb < 2; ++bb) {
        const u32x2 ho = *reinterpret_cast<const u32x2*>(t2 + bb * 320 + 40 * wave + 2 * g4);
        const unsigned hs = reinterpret_cast<const u16*>(t2)[bb * 640 + 80 * wave + 64 + g4];
#pragma unroll
        for (int k = 0; k < 8; ++k) a[bb * 8 + k] = dot2(w1p[16 + k], hs, dot2(w1p[8 + k], ho[1], dot2(w1p[k], ho[0], 0.f)));
      }
      const f32x4 s4 = red16_g4(a);
      if ((lane & 12) == 0 && c4 < 3) *reinterpret_cast<f32x4*>(qp + wave * 48 + (lane >> 5) * 24 + c4 * 8 + ((lane >> 4) & 1) * 4) = s4;
    }
    float mb = 0.f, rd = 0.f;
    if (wave < 5) {
#pragma unroll
      for (int w = 0; w < NW; ++w) csum += reinterpret_cast<const float*>(lds + L_CS)[w * 24 + li - 24 * lb];
      f32x2 sg[5];
#pragma unroll
      for (int k = 0; k < 5; ++k) sg[k] = *reinterpret_cast<const f32x2*>(lds + L_ST + (lb * 5 + k) * 8);
      const float mean = ((sg[0][0] + sg[1][0]) + (sg[2][0] + sg[3][0]) + sg[4][0]) * 0.2f;
      float m2 = (sg[0][1] + sg[1][1]) + (sg[2][1] + sg[3][1]) + sg[4][1], dd = 0.f;
#pragma unroll
      for (int k = 0; k < 5; ++k) { const float d = sg[k][0] - mean; dd = fmaf(d, d, dd); }
      const float var = fmaf(dd, 128.f, m2) * (1.0f / DM);
      mb = rbf(mean);
      rd = __builtin_amdgcn_rcpf(rbf(__builtin_amdgcn_sqrtf(rbf(rbf(var) + 1e-5f))));
    }
    lds_barrier();
    if (wave < 5) {
      float s = 0.f;
#pragma unroll
      for (int w = 0; w < NW; ++w) s += qp[w * 48 + li];
      const float hv = rbf(rbf(fmaf(-mb, csum, s) * rd) + bf1(bsm));
      const float rr = hv > 0.f ? hv : 0.f;
      const float r2 = rr * rr;
      const unsigned rp2 = pack2(r2, dpp<XOR1>(r2));
      float a00 = 0.f, a01 = 0.f, a10 = 0.f, a11 = 0.f;
#pragma unroll
      for (int i = 0; i < 12; ++i) {
        const unsigned p0 = (unsigned)__builtin_amdgcn_readlane((int)rp2, 2 * i), p1 = (unsigned)__builtin_amdgcn_readlane((int)rp2, 24 + 2 * i);
        a00 = dot2(w2v[2 * i], p0, a00); a01 = dot2(w2v[2 * i + 1], p0, a01); a10 = dot2(w2v[2 * i], p1, a10); a11 = dot2(w2v[2 * i + 1], p1, a11);
      }
      put_unit(rs, W_O4 + (unsigned)jb * 10240 + (unsigned)tid * 16, __float_as_uint(a00), __float_as_uint(a01), tA, tB);
      put_unit(rs, W_O4 + (unsigned)jb * 10240 + (unsigned)(320 + tid) * 16, __float_as_uint(a10), __float_as_uint(a11), tA, tB);
    }
  }
  STAMP(8);
}

extern "C" void kernel_launch(void* const* d_in, const int* in_sizes, int n_in, void* d_out, int out_size, void* d_ws, size_t ws_size, hipStream_t stream) {
  fused_kernel<<<256, NTHR, 0, stream>>>((const u16*)d_in[0], (const u16*)d_in[1], (const u16*)d_in[2], (const u16*)d_in[3], (const u16*)d_in[4], (const u16*)d_in[6],
                                         (const u16*)d_in[7], (const u16*)d_in[8], (const u16*)d_in[9], (u16*)d_out, (unsigned char*)d_ws);
}
```

```cpp
#include <hip/hip_runtime.h>
#include <hip/hip_bf16.h>
#include <stdint.h>

typedef unsigned u32x4 __attribute__((ext_vector_type(4)));
typedef unsigned u32x2 __attribute__((ext_vector_type(2)));
typedef float f32x4 __attribute__((ext_vector_type(4)));
typedef float f32x2 __attribute__((ext_vector_type(2)));
typedef __bf16 bf16x2 __attribute__((ext_vector_type(2)));
typedef unsigned short u16;
typedef unsigned long long u64;

#define STAMPS 0
#define PD 3

#define NSLOT 3
#define NS0 256
#define NS1 256
#define THR 40
#define CSE 1
#define RW 7
#define XSTEP 0
#define LATE 1
constexpr int DM = 640, NH = 32, SK = 8192, DH = 192, FF = 1536;
constexpr int NW = 8;
constexpr int NTHR = NW * 64;
constexpr int NRED = 160, NMLP = 64;
constexpr int RINGW = 18432;
constexpr int L_RING = 0;
constexpr int L_QP = NW * RINGW;
constexpr int L_Q = L_QP + 1536;
constexpr int L_MISC = L_Q + 384;
constexpr int L_T1 = L_MISC + 64;
constexpr int L_T2 = L_T1 + 8192;
constexpr int L_CS = L_T2 + 2560;
constexpr int L_ST = L_CS + 768;
constexpr int L_END = L_ST + 128;
static_assert(L_END <= 163840, "lds");
constexpr unsigned W_EPOCH = 0;
constexpr unsigned W_DN = 4096;
constexpr unsigned W_QX = 8192;
constexpr unsigned W_P1 = 262144;
constexpr unsigned W_Y2 = W_P1 + 256 * 1792;
constexpr unsigned W_H3 = W_Y2 + 256 * 1280;
constexpr unsigned W_O4 = W_H3 + 5120;
constexpr unsigned W_END = W_O4 + 64 * 10240;
constexpr unsigned W_STAMP = 4u << 20;
constexpr unsigned SPIN_CAP = 400000;
constexpr int NSTAMP = 10;

__device__ __forceinline__ float rbf(float x) { return (float)(__bf16)x; }
__device__ __forceinline__ float bflo(unsigned v) { return __builtin_bit_cast(float, v << 16); }
__device__ __forceinline__ float bfhi(unsigned v) { return __builtin_bit_cast(float, v & 0xffff0000u); }
__device__ __forceinline__ float bf1(unsigned v) { return __builtin_bit_cast(float, v << 16); }
__device__ __forceinline__ unsigned pack2(float lo, float hi) { bf16x2 v = {(__bf16)lo, (__bf16)hi}; return __builtin_bit_cast(unsigned, v); }
__device__ __forceinline__ float dot2(unsigned k, unsigned q, float acc) {
  return __builtin_amdgcn_fdot2_f32_bf16(__builtin_bit_cast(bf16x2, k), __builtin_bit_cast(bf16x2, q), acc, false);
}
__device__ __forceinline__ unsigned plo(unsigned r0, unsigned r1) { return __builtin_amdgcn_perm(r1, r0, 0x05040100u); }
__device__ __forceinline__ unsigned phi(unsigned r0, unsigned r1) { return __builtin_amdgcn_perm(r1, r0, 0x07060302u); }
template <int CTRL> __device__ __forceinline__ float dpp(float x) {
  return __builtin_bit_cast(float, __builtin_amdgcn_mov_dpp(__builtin_bit_cast(int, x), CTRL, 0xf, 0xf, true));
}
constexpr int XOR1 = 0xB1, XOR2 = 0x4E, HMIR = 0x141, MIR = 0x140, ROR8 = 0x128, ROR4 = 0x124;
__device__ __forceinline__ float sw32(float a, float b) {
  auto s = __builtin_amdgcn_permlane32_swap(__float_as_uint(a), __float_as_uint(b), false, false);
  return __uint_as_float(s[0]) + __uint_as_float(s[1]);
}
__device__ __forceinline__ float sw16(float a, float b) {
  auto s = __builtin_amdgcn_permlane16_swap(__float_as_uint(a), __float_as_uint(b), false, false);
  return __uint_as_float(s[0]) + __uint_as_float(s[1]);
}
__device__ __forceinline__ float xrow_max(float x) {
  auto s = __builtin_amdgcn_permlane16_swap(__float_as_uint(x), __float_as_uint(x), false, false);
  x = fmaxf(__uint_as_float(s[0]), __uint_as_float(s[1]));
  auto t = __builtin_amdgcn_permlane32_swap(__float_as_uint(x), __float_as_uint(x), false, false);
  return fmaxf(__uint_as_float(t[0]), __uint_as_float(t[1]));
}
__device__ __forceinline__ float grp8_sum(float x) { x += dpp<XOR1>(x); x += dpp<XOR2>(x); x += dpp<HMIR>(x); return x; }
__device__ __forceinline__ void lds_barrier() { asm volatile("s_waitcnt lgkmcnt(0)" ::: "memory"); __builtin_amdgcn_s_barrier(); asm volatile("" ::: "memory"); }
__device__ __forceinline__ float rdlane(float x, int l) { return __builtin_bit_cast(float, __builtin_amdgcn_readlane(__builtin_bit_cast(int, x), l)); }

__device__ __forceinline__ f32x2 wave_sum2(float a, float b) {
  float t = sw32(a, b);
  t = sw16(t, t);
  t = grp8_sum(t);
  t += dpp<MIR>(t);
  return f32x2{rdlane(t, 0), rdlane(t, 32)};
}
__device__ __forceinline__ void ln_stats2(const unsigned (&r)[2][5], float (&mb)[2], float (&rd)[2]) {
  float s[2] = {0.f, 0.f};
#pragma unroll
  for (int i = 0; i < 5; ++i) { s[0] = dot2(r[0][i], 0x3f803f80u, s[0]); s[1] = dot2(r[1][i], 0x3f803f80u, s[1]); }
  const f32x2 mean = wave_sum2(s[0], s[1]) * (1.0f / DM);
  f32x2 q[2] = {{0.f, 0.f}, {0.f, 0.f}};
#pragma unroll
  for (int i = 0; i < 5; ++i)
#pragma unroll
    for (int bb = 0; bb < 2; ++bb) { const f32x2 d = f32x2{bflo(r[bb][i]), bfhi(r[bb][i])} - mean[bb]; q[bb] = __builtin_elementwise_fma(d, d, q[bb]); }
  const f32x2 var = wave_sum2(q[0][0] + q[0][1], q[1][0] + q[1][1]) * (1.0f / DM);
#pragma unroll
  for (int bb = 0; bb < 2; ++bb) {
    mb[bb] = rbf(mean[bb]);
    rd[bb] = __builtin_amdgcn_rcpf(rbf(__builtin_amdgcn_sqrtf(rbf(rbf(var[bb]) + 1e-5f))));
  }
}
__device__ __forceinline__ unsigned ln_pair(unsigned v, float mb, float rd) {
  const unsigned d = pack2(bflo(v) - mb, bfhi(v) - mb);
  return pack2(bflo(d) * rd, bfhi(d) * rd);
}
__device__ __forceinline__ unsigned ln_one(unsigned v, float mb, float rd) { return pack2(rbf(bf1(v) - mb) * rd, 0.f); }

__device__ __forceinline__ void dma_half(unsigned lds, const char* gb, unsigned voff) {
  unsigned keep;
  asm volatile(
      "s_mov_b32 %0, m0\n\t"
      "s_mov_b32 m0, %3\n\t"
      "s_nop 0\n\t"
      "global_load_lds_dwordx4 %1, %2 nt\n\t"
      "s_add_u32 m0, m0, 0x380\n\t"
      "s_nop 0\n\t"
      "global_load_lds_dwordx4 %1, %2 offset:128 nt\n\t"
      "s_add_u32 m0, m0, 0x380\n\t"
      "s_nop 0\n\t"
      "global_load_lds_dwordx4 %1, %2 offset:256 nt\n\t"
      "s_mov_b32 m0, %0"
      : "=&s"(keep) : "v"(voff), "s"(gb), "s"(lds) : "memory", "scc");
}
__device__ __forceinline__ void dma_step(unsigned lds, const char* kb, const char* vb, unsigned voff) {
  unsigned keep;
  asm volatile(
      "s_waitcnt lgkmcnt(0)\n\t"
      "s_mov_b32 %0, m0\n\t"
      "s_mov_b32 m0, %4\n\t"
      "s_nop 0\n\t"
      "global_load_lds_dwordx4 %1, %2 nt\n\t"
      "s_add_u32 m0, m0, 0x380\n\t"
      "s_nop 0\n\t"
      "global_load_lds_dwordx4 %1, %2 offset:128 nt\n\t"
      "s_add_u32 m0, m0, 0x380\n\t"
      "s_nop 0\n\t"
      "global_load_lds_dwordx4 %1, %2 offset:256 nt\n\t"
      "s_add_u32 m0, m0, 0x500\n\t"
      "s_nop 0\n\t"
      "global_load_lds_dwordx4 %1, %3 nt\n\t"
      "s_add_u32 m0, m0, 0x380\n\t"
      "s_nop 0\n\t"
      "global_load_lds_dwordx4 %1, %3 offset:128 nt\n\t"
      "s_add_u32 m0, m0, 0x380\n\t"
      "s_nop 0\n\t"
      "global_load_lds_dwordx4 %1, %3 offset:256 nt\n\t"
      "s_mov_b32 m0, %0"
      : "=&s"(keep) : "v"(voff), "s"(kb), "s"(vb), "s"(lds) : "memory", "scc");
}
#define LD4NT(dst, voff, base, off) asm volatile("global_load_dwordx4 %0, %1, %2 offset:%c3 nt" : "=v"(dst) : "v"(voff), "s"(base), "i"(off) : "memory")
#define LD4NTP(dst, voff, base, off) asm volatile("global_load_dwordx4 %0, %1, %2 offset:%c3 nt" : "+v"(dst) : "v"(voff), "s"(base), "i"(off) : "memory")
#define LD1NT(dst, voff, base, off) asm volatile("global_load_dword %0, %1, %2 offset:%c3 nt" : "=v"(dst) : "v"(voff), "s"(base), "i"(off) : "memory")
#define LD4(dst, voff, base, off) asm volatile("global_load_dwordx4 %0, %1, %2 offset:%c3" : "=v"(dst) : "v"(voff), "s"(base), "i"(off) : "memory")
#define LD2(dst, voff, base, off) asm volatile("global_load_dwordx2 %0, %1, %2 offset:%c3" : "=v"(dst) : "v"(voff), "s"(base), "i"(off) : "memory")
#define LD1(dst, voff, base, off) asm volatile("global_load_dword %0, %1, %2 offset:%c3" : "=v"(dst) : "v"(voff), "s"(base), "i"(off) : "memory")
#define LDU16(dst, voff, base, off) asm volatile("global_load_ushort %0, %1, %2 offset:%c3" : "=v"(dst) : "v"(voff), "s"(base), "i"(off) : "memory")
#define WAITVM(n) asm volatile("s_waitcnt vmcnt(" #n ")" ::: "memory")
#define WAITVMX(n) WAITVM(n)

__device__ __forceinline__ void put_unit(__amdgpu_buffer_rsrc_t rs, unsigned off, unsigned v0, unsigned v1, unsigned tA, unsigned tB) {
  u32x4 u = {tA ^ v1, v0, tB ^ v0, v1};
  __builtin_amdgcn_raw_buffer_store_b128(u, rs, off, 0, 16);
}
__device__ __forceinline__ bool unit_ok(const u32x4& v, unsigned tA, unsigned tB) { return ((v[0] ^ v[3]) == tA) && ((v[2] ^ v[1]) == tB); }
template <int N>
__device__ __forceinline__ void get_units(__amdgpu_buffer_rsrc_t rs, const unsigned (&off)[N], u32x4 (&v)[N], bool active, unsigned tA, unsigned tB) {
  bool ok = !active;
#pragma nounroll
  for (unsigned spin = 0; spin < SPIN_CAP; ++spin) {
    if (!ok) {
      asm volatile("" ::: "memory");
#pragma unroll
      for (int k = 0; k < N; ++k) v[k] = __builtin_amdgcn_raw_buffer_load_b128(rs, off[k], 0, 16);
      bool g = true;
#pragma unroll
      for (int k = 0; k < N; ++k) g = g && unit_ok(v[k], tA, tB);
      ok = g;
    }
    if (__all(ok)) break;
    __builtin_amdgcn_s_sleep(1);
  }
}
template <int N>
__device__ __forceinline__ void get_units_sel(__amdgpu_buffer_rsrc_t rs, const unsigned (&off)[N], u32x4 (&v)[N], unsigned tA, unsigned tB) {
  bool ok[N];
#pragma unroll
  for (int k = 0; k < N; ++k) ok[k] = false;
#pragma nounroll
  for (unsigned spin = 0; spin < SPIN_CAP; ++spin) {
    asm volatile("" ::: "memory");
#pragma unroll
    for (int k = 0; k < N; ++k) if (!ok[k]) v[k] = __builtin_amdgcn_raw_buffer_load_b128(rs, off[k], 0, 16);
    bool g = true;
#pragma unroll
    for (int k = 0; k < N; ++k) { ok[k] = unit_ok(v[k], tA, tB); g = g && ok[k]; }
    if (__all(g)) break;
    __builtin_amdgcn_s_sleep(1);
  }
}
__device__ __forceinline__ f32x4 red16_g4(const float (&a)[16]) {
  float t[8], u[4];
#pragma unroll
  for (int k = 0; k < 8; ++k) t[k] = sw32(a[k], a[8 + k]);
#pragma unroll
  for (int k = 0; k < 4; ++k) { u[k] = sw16(t[k], t[k + 4]); u[k] += dpp<ROR8>(u[k]); u[k] += dpp<ROR4>(u[k]); }
  return f32x4{u[0], u[1], u[2], u[3]};
}

__global__ __launch_bounds__(NTHR) void fused_kernel(const u16* __restrict__ x, const u16* __restrict__ Kc, const u16* __restrict__ Vc, const u16* __restrict__ wq,
                                                      const u16* __restrict__ wo, const u16* __restrict__ w1, const u16* __restrict__ b1, const u16* __restrict__ w2,
                                                      const u16* __restrict__ b2, u16* __restrict__ out, unsigned char* __restrict__ ws) {
  __shared__ __attribute__((aligned(16))) unsigned char lds[L_END];
  const int bid = blockIdx.x, loc = bid >> 3;
  const int h = (bid & 7) * 4 + (loc >> 3), sx = loc & 7, b = sx >> 2, j = sx & 3, g = b * NH + h;
  const int tid = threadIdx.x, lane = tid & 63, wave = __builtin_amdgcn_readfirstlane(tid >> 6), r = lane >> 3, c = lane & 7;
  const unsigned lds0 = __builtin_amdgcn_readfirstlane((unsigned)(uintptr_t)(__attribute__((address_space(3))) unsigned char*)lds);
  const __amdgpu_buffer_rsrc_t rs = __builtin_amdgcn_make_buffer_rsrc(ws, 0, W_END, 0x00020000);
  float* qp = reinterpret_cast<float*>(lds + L_QP);
  unsigned* ql = reinterpret_cast<unsigned*>(lds + L_Q);
  unsigned* misc = reinterpret_cast<unsigned*>(lds + L_MISC);
  float* t1 = reinterpret_cast<float*>(lds + L_T1);
  unsigned* t2 = reinterpret_cast<unsigned*>(lds + L_T2);
#define STAMP(i)
  STAMP(0);
  const bool is_mlp = j == 0, is_op = j != 0 && sx != 7, is_red = is_op && wave == RW;
  const int jb = g, oi = sx - 1 - b, rid = h * 5 + oi;
  const int s0 = j == 0 ? 0 : NS0 + (j - 1) * NS1, nstep = j == 0 ? NS0 : (j == 3 ? 1024 - NS0 - 2 * NS1 : NS1);
  const char* kblk = reinterpret_cast<const char*>(Kc) + ((size_t)g * SK + (size_t)s0 * 8) * 384;
  const char* vblk = reinterpret_cast<const char*>(Vc) + ((size_t)g * SK + (size_t)s0 * 8) * 384;
  const unsigned ring = lds0 + L_RING + wave * RINGW;
  const unsigned kvoff = r * 384 + c * 16;
  const int g4 = lane >> 2, c4 = lane & 3, rg = lane >> 4, c16 = lane & 15;
  u32x2 ep;
  if (wave == NW - 1) {
    asm volatile("global_load_dwordx2 %0, %1, %2 sc1" : "=v"(ep) : "v"(0u), "s"(ws) : "memory");
    if (lane == 0) { misc[5] = 0; misc[6] = 0; }
  }
  const int qi = (0x2F41F530 >> (4 * sx)) & 15;
  if (qi < 6) {
    u32x4 xa[2], wv[5];
    unsigned xb[2], xs[2];
    u32x2 xo[2];
    const int k0 = (qi >= 3 ? 320 : 0) + 40 * wave;
    const unsigned o16 = lane * 16, o4 = lane * 4, oo = (k0 + 4 * r) * 2, os = (k0 + 32 + r) * 2;
    LD4(xa[0], o16, x, 0); LD1(xb[0], o4, x, 1024); LD4(xa[1], o16, x, 1280); LD1(xb[1], o4, x, 2304);
    LD2(xo[0], oo, x, 0); LDU16(xs[0], os, x, 0); LD2(xo[1], oo, x, 1280); LDU16(xs[1], os, x, 1280);
    const unsigned wcol = (unsigned)(h * 384 + (qi >= 3 ? qi - 3 : qi) * 128 + c * 16);
    const unsigned wa = (unsigned)(k0 + 4 * r) * 12288u + wcol, wb = (unsigned)(k0 + 32 + r) * 12288u + wcol;
    const char* wqc = reinterpret_cast<const char*>(wq);
    LD4NT(wv[0], wa, wqc, 0); LD4NT(wv[1], wa, wqc + 12288, 0); LD4NT(wv[2], wa, wqc + 24576, 0); LD4NT(wv[3], wa, wqc + 36864, 0); LD4NT(wv[4], wb, wqc, 0);
    dma_half(ring, kblk + (size_t)wave * 3072, kvoff);
    dma_half(ring + 3072, vblk + (size_t)wave * 3072, kvoff);
    asm volatile("s_waitcnt vmcnt(11) ; XIN_LANDED %0 %1 %2 %3 %4 %5 %6 %7" : "+v"(xa[0]), "+v"(xa[1]), "+v"(xb[0]), "+v"(xb[1]), "+v"(xo[0]), "+v"(xo[1]), "+v"(xs[0]), "+v"(xs[1])::"memory");
    const unsigned xr[2][5] = {{xa[0][0], xa[0][1], xa[0][2], xa[0][3], xb[0]}, {xa[1][0], xa[1][1], xa[1][2], xa[1][3], xb[1]}};
    float mb[2], rd[2];
    ln_stats2(xr, mb, rd);
    unsigned xp[2][3];
#pragma unroll
    for (int bb = 0; bb < 2; ++bb) { xp[bb][0] = ln_pair(xo[bb][0], mb[bb], rd[bb]); xp[bb][1] = ln_pair(xo[bb][1], mb[bb], rd[bb]); xp[bb][2] = ln_one(xs[bb], mb[bb], rd[bb]); }
    asm volatile("s_waitcnt vmcnt(6) ; WQ_LANDED %0 %1 %2 %3 %4" : "+v"(wv[0]), "+v"(wv[1]), "+v"(wv[2]), "+v"(wv[3]), "+v"(wv[4]), "+v"(xp[0][0]), "+v"(xp[0][1]), "+v"(xp[0][2]), "+v"(xp[1][0]), "+v"(xp[1][1]), "+v"(xp[1][2])::"memory");
    STAMP(1);
    float a[16];
#pragma unroll
    for (int k = 0; k < 4; ++k) {
      const unsigned p0 = plo(wv[0][k], wv[1][k]), p1 = phi(wv[0][k], wv[1][k]), p2 = plo(wv[2][k], wv[3][k]), p3 = phi(wv[2][k], wv[3][k]);
      const unsigned p4 = wv[4][k] & 0xffffu, p5 = wv[4][k] >> 16;
#pragma unroll
      for (int bb = 0; bb < 2; ++bb) {
        a[bb * 8 + 2 * k] = dot2(p4, xp[bb][2], dot2(p2, xp[bb][1], dot2(p0, xp[bb][0], 0.f)));
        a[bb * 8 + 2 * k + 1] = dot2(p5, xp[bb][2], dot2(p3, xp[bb][1], dot2(p1, xp[bb][0], 0.f)));
      }
    }
    float t[8], u[4];
#pragma unroll
    for (int k = 0; k < 8; ++k) t[k] = sw32(a[k], a[8 + k]);
#pragma unroll
    for (int k = 0; k < 4; ++k) { u[k] = sw16(t[k], t[k + 4]); u[k] += dpp<ROR8>(u[k]); }
    if ((lane & 8) == 0) *reinterpret_cast<f32x4*>(t1 + wave * 128 + (lane >> 5) * 64 + c * 8 + ((lane >> 4) & 1) * 4) = f32x4{u[0], u[1], u[2], u[3]};
  } else {
    dma_half(ring, kblk + (size_t)wave * 3072, kvoff);
    dma_half(ring + 3072, vblk + (size_t)wave * 3072, kvoff);
    if (wave == NW - 1) WAITVM(6);
  }
  lds_barrier();
  STAMP(2);
  if (wave == NW - 1) {
    asm volatile("; EP_LANDED %0" : "+v"(ep));
    const u64 e1 = (((u64)ep[1] << 32) | ep[0]) + 1ull;
    const unsigned tA = (unsigned)e1, tB = (((unsigned)e1 * 0x9E3779B1u) ^ (unsigned)(e1 >> 32)) | 1u;
    if (qi < 6) {
      f32x2 a = {0.f, 0.f};
#pragma unroll
      for (int w = 0; w < NW; ++w) a += *reinterpret_cast<const f32x2*>(t1 + w * 128 + lane * 2);
      put_unit(rs, W_QX + (unsigned)((h * 6 + qi) * 64 + lane) * 16, __float_as_uint(a[0]), __float_as_uint(a[1]), tA, tB);
    }
    if (lane == 0) { misc[0] = (NSLOT + 3 + XSTEP) * NW; misc[1] = tA; misc[2] = tB; misc[3] = (unsigned)e1; misc[4] = (unsigned)(e1 >> 32); }
    unsigned off[3];
#pragma unroll
    for (int k = 0; k < 3; ++k) off[k] = W_QX + (unsigned)((h * 6 + (lane >> 5) * 3 + k) * 64 + b * 32 + (lane & 31)) * 16;
    u32x4 v[3];
    get_units_sel<3>(rs, off, v, tA, tB);
#pragma unroll
    for (int k = 0; k < 3; ++k) reinterpret_cast<u16*>(ql)[(k * 32 + (lane & 31)) * 2 + (lane >> 5)] = (u16)(pack2(sw32(__uint_as_float(v[k][1]), __uint_as_float(v[k][3])), 0.f) & 0xffffu);
    asm volatile("s_waitcnt lgkmcnt(0)" ::: "memory");
    if (lane == 0) __hip_atomic_store(misc + 5, 1u, __ATOMIC_RELAXED, __HIP_MEMORY_SCOPE_WORKGROUP);
    STAMP(9);
  }
  u32x4 wov[6];
  unsigned bsm = 0, rx = 0, rb2 = 0;
  const char* w1c = reinterpret_cast<const char*>(w1) + jb * 48;
  const char* w2c = reinterpret_cast<const char*>(w2) + (size_t)jb * 30720;
  {
    if (is_mlp) {
      const unsigned bo = (unsigned)(jb * 24 + (lane < 48 ? lane % 24 : 0)) * 2;
      LDU16(bsm, bo, b1, 0);
      LD1(rx, (unsigned)(lane & 1) * (639u * 3072u), w1c, 0);
      LD1(rb2, (unsigned)(lane & 1) * 30716u, w2c, 0);
    } else if (is_red) {
      const unsigned col = (unsigned)((rid % 80) * 8 + 2 * c4 + (lane >> 5));
      LDU16(rx, col * 2, x + (rid / 80) * DM, 0);
      LDU16(rb2, col * 2, b2, 0);
    }
    WAITVMX(PD); dma_half(ring + 6144, kblk + (size_t)(wave + 8) * 3072, kvoff);
    WAITVMX(PD); dma_half(ring + 9216, vblk + (size_t)(wave + 8) * 3072, kvoff);
    WAITVMX(PD); dma_half(ring + 12288, kblk + (size_t)(wave + 16) * 3072, kvoff);
    WAITVMX(PD); dma_half(ring + 15360, vblk + (size_t)(wave + 16) * 3072, kvoff);
    if (is_op) {
      const char* woc = reinterpret_cast<const char*>(wo) + (size_t)(h * DH + 24 * wave) * 1280 + oi * 256;
      const unsigned oa = (unsigned)(4 * rg) * 1280u + c16 * 16, ob = (unsigned)(16 + 2 * rg) * 1280u + c16 * 16;
      WAITVMX(PD); LD4NT(wov[0], oa, woc, 0); LD4NT(wov[1], oa, woc, 1280); LD4NT(wov[2], oa, woc, 2560);
      WAITVMX(PD); LD4NT(wov[3], oa, woc, 3840); LD4NT(wov[4], ob, woc, 0); LD4NT(wov[5], ob, woc, 1280);
    }
  }
  while (__hip_atomic_load(misc + 5, __ATOMIC_RELAXED, __HIP_MEMORY_SCOPE_WORKGROUP) == 0) __builtin_amdgcn_s_sleep(1);
  asm volatile("" ::: "memory");
  STAMP(3);
  const unsigned tA = misc[1], tB = misc[2];
  float m = -INFINITY, l = 0.f;
  float acc[24];
  unsigned wop[24];
  {
    u32x4 q[3];
#pragma unroll
    for (int t = 0; t < 3; ++t) q[t] = *reinterpret_cast<const u32x4*>(ql + t * 32 + c * 4);
#pragma unroll
    for (int k = 0; k < 24; ++k) acc[k] = 0.f;
    constexpr float SC = 0.07216878364870322f, L2E = 1.4426950408889634f;
    int nxt = (NSLOT + XSTEP) * NW + wave;
    int slot = 0, it = 0;
    const unsigned char* rp = lds + L_RING + wave * RINGW + lane * 16;
    auto step = [&](const u32x4 (&kb)[3], const u32x4 (&vb)[3]) {
      float d = 0.f;
#pragma unroll
      for (int t = 0; t < 3; ++t)
#pragma unroll
        for (int k = 0; k < 4; ++k) d = dot2(kb[t][k], q[t][k], d);
      d = grp8_sum(d);
      const float s_ = rbf(rbf(d) * SC) * L2E;
      if (__any(s_ > m)) {
        const float mx = fmaxf(m, s_), al = __builtin_amdgcn_exp2f(m - mx);
        l *= al;
#pragma unroll
        for (int k = 0; k < 24; ++k) acc[k] *= al;
        m = mx;
      }
      const float p = __builtin_amdgcn_exp2f(s_ - m);
      l += p;
#pragma unroll
      for (int t = 0; t < 3; ++t)
#pragma unroll
        for (int k = 0; k < 4; ++k) {
          acc[t * 8 + 2 * k] = fmaf(p, bflo(vb[t][k]), acc[t * 8 + 2 * k]);
          acc[t * 8 + 2 * k + 1] = fmaf(p, bfhi(vb[t][k]), acc[t * 8 + 2 * k + 1]);
        }
    };
#pragma nounroll
    for (; nxt < nstep; ++it) {
      asm volatile("s_waitcnt vmcnt(12)" ::: "memory");
      u32x4 kb[3], vb[3];
#pragma unroll
      for (int t = 0; t < 3; ++t) { kb[t] = *reinterpret_cast<const u32x4*>(rp + slot * 6144 + t * 1024); vb[t] = *reinterpret_cast<const u32x4*>(rp + slot * 6144 + 3072 + t * 1024); }
      dma_step(ring + slot * 6144, kblk + (size_t)nxt * 3072, vblk + (size_t)nxt * 3072, kvoff);
      if (it < 2) nxt += NW;
      else {
        int n2 = 0;
        if (lane == 0) n2 = __hip_atomic_fetch_add(misc, 1u, __ATOMIC_RELAXED, __HIP_MEMORY_SCOPE_WORKGROUP);
        nxt = __builtin_amdgcn_readfirstlane(n2);
      }
      step(kb, vb);
      if (it == 2) {
        asm volatile("s_waitcnt vmcnt(18) ; X_LANDED %0 %1 %2 %3 %4 %5 %6 %7 %8" : "+v"(bsm), "+v"(rx), "+v"(rb2), "+v"(wov[0]), "+v"(wov[1]), "+v"(wov[2]), "+v"(wov[3]), "+v"(wov[4]), "+v"(wov[5])::"memory");
#pragma unroll
        for (int pr = 0; pr < 3; ++pr)
#pragma unroll
          for (int k = 0; k < 4; ++k) { wop[pr * 8 + 2 * k] = plo(wov[2 * pr][k], wov[2 * pr + 1][k]); wop[pr * 8 + 2 * k + 1] = phi(wov[2 * pr][k], wov[2 * pr + 1][k]); }
      }
      slot = slot == NSLOT - 1 ? 0 : slot + 1;
    }
#pragma nounroll
    for (int wc = 2; wc >= 0; --wc) {
      if (wc == 2) asm volatile("s_waitcnt vmcnt(12)" ::: "memory");
      else if (wc == 1) asm volatile("s_waitcnt vmcnt(6)" ::: "memory");
      else asm volatile("s_waitcnt vmcnt(0)" ::: "memory");
      u32x4 kb[3], vb[3];
#pragma unroll
      for (int t = 0; t < 3; ++t) { kb[t] = *reinterpret_cast<const u32x4*>(rp + slot * 6144 + t * 1024); vb[t] = *reinterpret_cast<const u32x4*>(rp + slot * 6144 + 3072 + t * 1024); }
      step(kb, vb);
      slot = slot == NSLOT - 1 ? 0 : slot + 1;
    }
    STAMP(4);
    const float M = xrow_max(fmaxf(m, dpp<ROR8>(m)));
    const float al = __builtin_amdgcn_exp2f(m - M);
    l *= al; l += dpp<ROR8>(l); l = sw16(l, l); l = sw32(l, l);
    float t12[12], t6[6];
#pragma unroll
    for (int k = 0; k < 12; ++k) t12[k] = sw32(acc[k] * al, acc[k + 12] * al);
#pragma unroll
    for (int k = 0; k < 6; ++k) { t6[k] = sw16(t12[k], t12[k + 6]); t6[k] += dpp<ROR8>(t6[k]); }
    float* rec = reinterpret_cast<float*>(lds + L_RING + wave * RINGW);
    if ((lane & 8) == 0) {
      float* p = rec + 4 + c * 24 + 12 * (lane >> 5) + 6 * ((lane >> 4) & 1);
#pragma unroll
      for (int k = 0; k < 3; ++k) *reinterpret_cast<f32x2*>(p + 2 * k) = f32x2{t6[2 * k], t6[2 * k + 1]};
      if (lane == 0) *reinterpret_cast<f32x2*>(rec) = f32x2{M, l};
    }
  }
  lds_barrier();
  if (tid < 97) {
    const int d = 2 * tid, idx = tid < 96 ? 4 + ((d & 63) >> 3) * 24 + (d >> 6) * 8 + (d & 7) : 0;
    f32x2 ml[NW], a[NW];
#pragma unroll
    for (int w = 0; w < NW; ++w) {
      const float* rec = reinterpret_cast<const float*>(lds + L_RING + w * RINGW);
      ml[w] = *reinterpret_cast<const f32x2*>(rec); a[w] = *reinterpret_cast<const f32x2*>(rec + idx);
    }
    float M = ml[0][0];
#pragma unroll
    for (int w = 1; w < NW; ++w) M = fmaxf(M, ml[w][0]);
    f32x2 o = {0.f, 0.f};
#pragma unroll
    for (int w = 0; w < NW; ++w) { const float wt = __builtin_amdgcn_exp2f(ml[w][0] - M); o = __builtin_elementwise_fma(f32x2{wt, wt}, a[w], o); }
    put_unit(rs, W_P1 + (unsigned)(h * 8 + sx) * 1792 + tid * 16, __float_as_uint(tid < 96 ? o[0] : M), __float_as_uint(o[1]), tA, tB);
  }
  if (tid == 128) put_unit(rs, W_DN + (unsigned)bid * 16, 0u, 0u, tA, tB);
  if (is_op) {
  if (wave >= 5) {
    const int t3 = tid - 320, bb = t3 >= 96, u = t3 - 96 * bb;
    const unsigned base = W_P1 + (unsigned)(h * 8 + bb * 4) * 1792;
    u32x4 v[4], f[4];
#pragma unroll
    for (int jj = 0; jj < 4; ++jj) { f[jj] = __builtin_amdgcn_raw_buffer_load_b128(rs, base + jj * 1792 + 96 * 16, 0, 16); v[jj] = __builtin_amdgcn_raw_buffer_load_b128(rs, base + jj * 1792 + u * 16, 0, 16); }
#pragma nounroll
    for (unsigned spin = 0; spin < SPIN_CAP; ++spin) {
      bool okf[4], okv[4];
      int miss = 0;
      bool all = true;
#pragma unroll
      for (int jj = 0; jj < 4; ++jj) { okf[jj] = unit_ok(f[jj], tA, tB); okv[jj] = unit_ok(v[jj], tA, tB); miss += okf[jj] ? 0 : 1; all = all && okf[jj] && okv[jj]; }
      if (__all(all)) break;
      __builtin_amdgcn_s_sleep(1);
      asm volatile("" ::: "memory");
#pragma unroll
      for (int jj = 0; jj < 4; ++jj) {
        if (!okf[jj]) f[jj] = __builtin_amdgcn_raw_buffer_load_b128(rs, base + jj * 1792 + 96 * 16, 0, 16);
        if (!okv[jj] && (okf[jj] || miss <= 1)) v[jj] = __builtin_amdgcn_raw_buffer_load_b128(rs, base + jj * 1792 + u * 16, 0, 16);
      }
    }
    const float M = fmaxf(fmaxf(__uint_as_float(f[0][1]), __uint_as_float(f[1][1])), fmaxf(__uint_as_float(f[2][1]), __uint_as_float(f[3][1])));
    float L = 0.f, o0 = 0.f, o1 = 0.f;
#pragma unroll
    for (int jj = 0; jj < 4; ++jj) {
      const float wt = __builtin_amdgcn_exp2f(__uint_as_float(f[jj][1]) - M);
      L = fmaf(wt, __uint_as_float(f[jj][3]), L); o0 = fmaf(wt, __uint_as_float(v[jj][1]), o0); o1 = fmaf(wt, __uint_as_float(v[jj][3]), o1);
    }
    const float rl = __builtin_amdgcn_rcpf(L);
    t2[t3] = pack2(o0 * rl, o1 * rl);
  }
  lds_barrier();
  STAMP(5);
  {
    float a[16];
#pragma unroll
    for (int bb = 0; bb < 2; ++bb) {
      const u32x2 p01 = *reinterpret_cast<const u32x2*>(t2 + bb * 96 + 12 * wave + 2 * rg);
      const unsigned p2 = t2[bb * 96 + 12 * wave + 8 + rg];
#pragma unroll
      for (int k = 0; k < 8; ++k) a[bb * 8 + k] = dot2(wop[16 + k], p2, dot2(wop[8 + k], p01[1], dot2(wop[k], p01[0], 0.f)));
    }
    float t[8];
#pragma unroll
    for (int k = 0; k < 8; ++k) t[k] = sw32(a[k], a[8 + k]);
    const f32x4 s4 = {sw16(t[0], t[4]), sw16(t[1], t[5]), sw16(t[2], t[6]), sw16(t[3], t[7])};
    *reinterpret_cast<f32x4*>(t1 + wave * 256 + (lane >> 5) * 128 + c16 * 8 + ((lane >> 4) & 1) * 4) = s4;
  }
  lds_barrier();
  if (tid < 128) {
    f32x2 a = {0.f, 0.f};
#pragma unroll
    for (int w = 0; w < NW; ++w) a += *reinterpret_cast<const f32x2*>(t1 + w * 256 + 2 * tid);
    put_unit(rs, W_Y2 + (unsigned)(h * 5 + oi) * 2048 + tid * 16, __float_as_uint(a[0]), __float_as_uint(a[1]), tA, tB);
  }
  STAMP(6);
    if (is_red) {
      const int bq = rid / 80, cg = rid % 80;
      float hv;
      {
        unsigned off[2];
#pragma unroll
        for (int k = 0; k < 2; ++k) off[k] = W_Y2 + (unsigned)((16 * k + g4) * 5 + (cg >> 4)) * 2048 + (unsigned)(bq * 64 + (cg & 15) * 4 + c4) * 16;
        u32x4 v[2];
        get_units<2>(rs, off, v, true, tA, tB);
        const float a0 = __uint_as_float(v[0][1]) + __uint_as_float(v[1][1]);
        const float a1 = __uint_as_float(v[0][3]) + __uint_as_float(v[1][3]);
        float s = sw32(a0, a1); s = sw16(s, s); s += dpp<ROR8>(s); s += dpp<ROR4>(s);
        hv = rbf(bf1(rx) + rbf(s));
        auto e = __builtin_amdgcn_permlane32_swap(__float_as_uint(hv), __float_as_uint(hv), false, false);
        const unsigned pk = (e[0] >> 16) | (e[1] & 0xffff0000u);
        const unsigned pn = (unsigned)__builtin_amdgcn_mov_dpp((int)pk, XOR1, 0xf, 0xf, true);
        if (lane < 4 && !(lane & 1)) put_unit(rs, W_H3 + (unsigned)(rid * 2 + (lane >> 1)) * 16, pk, pn, tA, tB);
      }
      STAMP(7);
      {
        unsigned off[4];
#pragma unroll
        for (int k = 0; k < 4; ++k) off[k] = W_O4 + (unsigned)(16 * k + g4) * 10240 + (unsigned)(bq * 320 + cg * 4 + c4) * 16;
        u32x4 v[4];
        get_units<4>(rs, off, v, true, tA, tB);
        const float a0 = (__uint_as_float(v[0][1]) + __uint_as_float(v[1][1])) + (__uint_as_float(v[2][1]) + __uint_as_float(v[3][1]));
        const float a1 = (__uint_as_float(v[0][3]) + __uint_as_float(v[1][3])) + (__uint_as_float(v[2][3]) + __uint_as_float(v[3][3]));
        float s = sw32(a0, a1); s = sw16(s, s); s += dpp<ROR8>(s); s += dpp<ROR4>(s);
        const float ov = rbf(hv + rbf(rbf(s) + bf1(rb2)));
        auto e = __builtin_amdgcn_permlane32_swap(__float_as_uint(ov), __float_as_uint(ov), false, false);
        if (lane < 4) reinterpret_cast<unsigned*>(out)[bq * 320 + cg * 4 + lane] = (e[0] >> 16) | (e[1] & 0xffff0000u);
        if (rid == 0 && lane == 0) __hip_atomic_store(reinterpret_cast<u64*>(ws + W_EPOCH), ((u64)misc[4] << 32) | misc[3], __ATOMIC_RELAXED, __HIP_MEMORY_SCOPE_AGENT);
      }
    }
  } else if (is_mlp) {
    const int li = lane < 48 ? lane : 0, lb = li >= 24;
    float csum = 0.f;
    unsigned w1p[24], w2v[24];
    {
      if (LATE) {
        if (wave == 7) {
          const unsigned smo = W_DN + (unsigned)((bid & 192) + lane) * 16;
#pragma nounroll
          for (unsigned spin = 0; spin < SPIN_CAP; ++spin) {
            const u32x4 v = __builtin_amdgcn_raw_buffer_load_b128(rs, smo, 0, 16);
            if (__builtin_popcountll(__ballot(unit_ok(v, tA, tB))) >= THR) break;
            __builtin_amdgcn_s_sleep(4);
          }
          if (lane == 0) __hip_atomic_store(misc + 6, 1u, __ATOMIC_RELAXED, __HIP_MEMORY_SCOPE_WORKGROUP);
        } else {
          while (__hip_atomic_load(misc + 6, __ATOMIC_RELAXED, __HIP_MEMORY_SCOPE_WORKGROUP) == 0) __builtin_amdgcn_s_sleep(1);
        }
      }
      u32x4 w1v[5];
      const unsigned w1a = (unsigned)(80 * wave + 4 * g4) * 3072u + (c4 < 3 ? c4 : 2) * 16, w1b = (unsigned)(80 * wave + 64 + g4) * 3072u + (c4 < 3 ? c4 : 2) * 16;
      LD4NT(w1v[0], w1a, w1c, 0); LD4NT(w1v[1], w1a, w1c, 3072); LD4NT(w1v[2], w1a, w1c + 6144, 0); LD4NT(w1v[3], w1a, w1c + 6144, 3072); LD4NT(w1v[4], w1b, w1c, 0);
      const unsigned o2 = (unsigned)(tid < 320 ? tid : 0) * 4;
      if (wave < 5) {
#pragma unroll
        for (int k = 0; k < 24; k += 3) { LD1NT(w2v[k], o2, w2c + k * 1280, 0); LD1NT(w2v[k + 1], o2, w2c + k * 1280, 1280); LD1NT(w2v[k + 2], o2, w2c + k * 1280, 2560); }
        asm volatile("s_waitcnt vmcnt(24)" ::: "memory");
      } else {
        asm volatile("s_waitcnt vmcnt(0)" ::: "memory");
      }
      asm volatile("; W1_LANDED %0 %1 %2 %3 %4" : "+v"(w1v[0]), "+v"(w1v[1]), "+v"(w1v[2]), "+v"(w1v[3]), "+v"(w1v[4])::"memory");
      STAMP(5);
#pragma unroll
      for (int k = 0; k < 4; ++k) {
        w1p[2 * k] = plo(w1v[0][k], w1v[1][k]); w1p[2 * k + 1] = phi(w1v[0][k], w1v[1][k]);
        w1p[8 + 2 * k] = plo(w1v[2][k], w1v[3][k]); w1p[8 + 2 * k + 1] = phi(w1v[2][k], w1v[3][k]);
        w1p[16 + 2 * k] = w1v[4][k] & 0xffffu; w1p[16 + 2 * k + 1] = w1v[4][k] >> 16;
      }
      float cs[8];
#pragma unroll
      for (int k = 0; k < 8; ++k) cs[k] = dot2(w1p[16 + k], 0x3f803f80u, dot2(w1p[8 + k], 0x3f803f80u, dot2(w1p[k], 0x3f803f80u, 0.f)));
      float c0 = sw16(sw32(cs[0], cs[4]), sw32(cs[2], cs[6])), c1 = sw16(sw32(cs[1], cs[5]), sw32(cs[3], cs[7]));
      c0 += dpp<ROR8>(c0); c0 += dpp<ROR4>(c0); c1 += dpp<ROR8>(c1); c1 += dpp<ROR4>(c1);
      if ((lane & 12) == 0 && c4 < 3) *reinterpret_cast<f32x2*>(lds + L_CS + (wave * 24 + c4 * 8 + 4 * (lane >> 5) + 2 * ((lane >> 4) & 1)) * 4) = f32x2{c0, c1};
      asm volatile("s_waitcnt vmcnt(0) ; W2_LANDED %0 %1 %2 %3 %4 %5 %6 %7 %8 %9 %10 %11 %12 %13 %14 %15 %16 %17 %18 %19 %20 %21 %22 %23" : "+v"(w2v[0]), "+v"(w2v[1]), "+v"(w2v[2]), "+v"(w2v[3]), "+v"(w2v[4]), "+v"(w2v[5]), "+v"(w2v[6]), "+v"(w2v[7]), "+v"(w2v[8]), "+v"(w2v[9]), "+v"(w2v[10]), "+v"(w2v[11]),
                   "+v"(w2v[12]), "+v"(w2v[13]), "+v"(w2v[14]), "+v"(w2v[15]), "+v"(w2v[16]), "+v"(w2v[17]), "+v"(w2v[18]), "+v"(w2v[19]), "+v"(w2v[20]), "+v"(w2v[21]), "+v"(w2v[22]), "+v"(w2v[23]));
      STAMP(6);
#pragma unroll
      for (int i = 0; i < 12; ++i) { const unsigned lo = plo(w2v[2 * i], w2v[2 * i + 1]), hi = phi(w2v[2 * i], w2v[2 * i + 1]); w2v[2 * i] = lo; w2v[2 * i + 1] = hi; }
      lds_barrier();
      if (wave < 5) {
#pragma unroll
        for (int w = 0; w < NW; ++w) csum += reinterpret_cast<const float*>(lds + L_CS)[w * 24 + li - 24 * lb];
      }
      asm volatile("" : "+v"(csum), "+v"(w2v[0]), "+v"(w2v[1]), "+v"(w2v[2]), "+v"(w2v[3]), "+v"(w2v[4]), "+v"(w2v[5]), "+v"(w2v[6]), "+v"(w2v[7]), "+v"(w2v[8]), "+v"(w2v[9]), "+v"(w2v[10]), "+v"(w2v[11]),
                   "+v"(w2v[12]), "+v"(w2v[13]), "+v"(w2v[14]), "+v"(w2v[15]), "+v"(w2v[16]), "+v"(w2v[17]), "+v"(w2v[18]), "+v"(w2v[19]), "+v"(w2v[20]), "+v"(w2v[21]), "+v"(w2v[22]), "+v"(w2v[23]));
    }
    if (wave < 5) {
      unsigned off[1] = {W_H3 + (unsigned)tid * 16};
      u32x4 v[1];
      get_units<1>(rs, off, v, true, tA, tB);
      *reinterpret_cast<u32x2*>(t2 + 2 * tid) = u32x2{v[0][1], v[0][3]};
      float s = dot2(v[0][1], 0x3f803f80u, dot2(v[0][3], 0x3f803f80u, 0.f));
      s = grp8_sum(s); s += dpp<MIR>(s); s = sw16(s, s);
      const float mu = s * (1.0f / 128.f);
      const f32x2 d0 = f32x2{bflo(v[0][1]), bfhi(v[0][1])} - mu, d1 = f32x2{bflo(v[0][3]), bfhi(v[0][3])} - mu;
      const f32x2 q2 = __builtin_elementwise_fma(d0, d0, d1 * d1);
      float q = q2[0] + q2[1];
      q = grp8_sum(q); q += dpp<MIR>(q); q = sw16(q, q);
      if ((lane & 31) == 0) *reinterpret_cast<f32x2*>(lds + L_ST + (tid >> 5) * 8) = f32x2{mu, q};
    }
    lds_barrier();
    STAMP(7);
    {
      float a[16];
#pragma unroll
      for (int bb = 0; bb < 2; ++bb) {
        const u32x2 ho = *reinterpret_cast<const u32x2*>(t2 + bb * 320 + 40 * wave + 2 * g4);
        const unsigned hs = reinterpret_cast<const u16*>(t2)[bb * 640 + 80 * wave + 64 + g4];
#pragma unroll
        for (int k = 0; k < 8; ++k) a[bb * 8 + k] = dot2(w1p[16 + k], hs, dot2(w1p[8 + k], ho[1], dot2(w1p[k], ho[0], 0.f)));
      }
      const f32x4 s4 = red16_g4(a);
      if ((lane & 12) == 0 && c4 < 3) *reinterpret_cast<f32x4*>(qp + wave * 48 + (lane >> 5) * 24 + c4 * 8 + ((lane >> 4) & 1) * 4) = s4;
    }
    float mb = 0.f, rd = 0.f;
    if (wave < 5) {
      f32x2 sg[5];
#pragma unroll
      for (int k = 0; k < 5; ++k) sg[k] = *reinterpret_cast<const f32x2*>(lds + L_ST + (lb * 5 + k) * 8);
      const float mean = ((sg[0][0] + sg[1][0]) + (sg[2][0] + sg[3][0]) + sg[4][0]) * 0.2f;
      float m2 = (sg[0][1] + sg[1][1]) + (sg[2][1] + sg[3][1]) + sg[4][1], dd = 0.f;
#pragma unroll
      for (int k = 0; k < 5; ++k) { const float d = sg[k][0] - mean; dd = fmaf(d, d, dd); }
      const float var = fmaf(dd, 128.f, m2) * (1.0f / DM);
      mb = rbf(mean);
      rd = __builtin_amdgcn_rcpf(rbf(__builtin_amdgcn_sqrtf(rbf(rbf(var) + 1e-5f))));
    }
    lds_barrier();
    if (wave < 5) {
      float s = 0.f;
#pragma unroll
      for (int w = 0; w < NW; ++w) s += qp[w * 48 + li];
      const float hv = rbf(rbf(fmaf(-mb, csum, s) * rd) + bf1(bsm));
      const float rr = hv > 0.f ? hv : 0.f;
      const float r2 = rr * rr;
      const unsigned rp2 = pack2(r2, dpp<XOR1>(r2));
      float a00 = 0.f, a01 = 0.f, a10 = 0.f, a11 = 0.f;
#pragma unroll
      for (int i = 0; i < 12; ++i) {
        const unsigned p0 = (unsigned)__builtin_amdgcn_readlane((int)rp2, 2 * i), p1 = (unsigned)__builtin_amdgcn_readlane((int)rp2, 24 + 2 * i);
        a00 = dot2(w2v[2 * i], p0, a00); a01 = dot2(w2v[2 * i + 1], p0, a01); a10 = dot2(w2v[2 * i], p1, a10); a11 = dot2(w2v[2 * i + 1], p1, a11);
      }
      put_unit(rs, W_O4 + (unsigned)jb * 10240 + (unsigned)tid * 16, __float_as_uint(a00), __float_as_uint(a01), tA, tB);
      put_unit(rs, W_O4 + (unsigned)jb * 10240 + (unsigned)(320 + tid) * 16, __float_as_uint(a10), __float_as_uint(a11), tA, tB);
    }
  }
  STAMP(8);
}

extern "C" void kernel_launch(void* const* d_in, const int* in_sizes, int n_in, void* d_out, int out_size, void* d_ws, size_t ws_size, hipStream_t stream) {
  fused_kernel<<<256, NTHR, 0, stream>>>((const u16*)d_in[0], (const u16*)d_in[1], (const u16*)d_in[2], (const u16*)d_in[3], (const u16*)d_in[4], (const u16*)d_in[6],
                                         (const u16*)d_in[7], (const u16*)d_in[8], (const u16*)d_in[9], (u16*)d_out, (unsigned char*)d_ws);
}
```

```cpp
#include <hip/hip_runtime.h>
#include <hip/hip_bf16.h>
#include <stdint.h>

typedef unsigned u32x4 __attribute__((ext_vector_type(4)));
typedef unsigned u32x2 __attribute__((ext_vector_type(2)));
typedef float f32x4 __attribute__((ext_vector_type(4)));
typedef float f32x2 __attribute__((ext_vector_type(2)));
typedef __bf16 bf16x2 __attribute__((ext_vector_type(2)));
typedef unsigned short u16;
typedef unsigned long long u64;

#define STAMPS 0
#define PD 3

#define NSLOT 3
#define PATM 0
#define ROTG 5
#define THR 40
#define CSE 1
#define RW 7
#define XSTEP 0
#define LATE 1
constexpr int DM = 640, NH = 32, SK = 8192, DH = 192, FF = 1536;
constexpr int NW = 8;
constexpr int NTHR = NW * 64;
constexpr int NRED = 160, NMLP = 64;
constexpr int RINGW = 18432;
constexpr int L_RING = 0;
constexpr int L_QP = NW * RINGW;
constexpr int L_Q = L_QP + 1536;
constexpr int L_MISC = L_Q + 384;
constexpr int L_T1 = L_MISC + 64;
constexpr int L_T2 = L_T1 + 8192;
constexpr int L_CS = L_T2 + 2560;
constexpr int L_ST = L_CS + 768;
constexpr int L_END = L_ST + 128;
static_assert(L_END <= 163840, "lds");
constexpr unsigned W_EPOCH = 0;
constexpr unsigned W_DN = 4096;
constexpr unsigned W_QX = 8192;
constexpr unsigned W_P1 = 262144;
constexpr unsigned W_Y2 = W_P1 + 256 * 1792;
constexpr unsigned W_H3 = W_Y2 + 256 * 1280;
constexpr unsigned W_O4 = W_H3 + 5120;
constexpr unsigned W_END = W_O4 + 64 * 10240;
constexpr unsigned W_STAMP = 4u << 20;
constexpr unsigned SPIN_CAP = 400000;
constexpr int NSTAMP = 10;

__device__ __forceinline__ float rbf(float x) { return (float)(__bf16)x; }
__device__ __forceinline__ float bflo(unsigned v) { return __builtin_bit_cast(float, v << 16); }
__device__ __forceinline__ float bfhi(unsigned v) { return __builtin_bit_cast(float, v & 0xffff0000u); }
__device__ __forceinline__ float bf1(unsigned v) { return __builtin_bit_cast(float, v << 16); }
__device__ __forceinline__ unsigned pack2(float lo, float hi) { bf16x2 v = {(__bf16)lo, (__bf16)hi}; return __builtin_bit_cast(unsigned, v); }
__device__ __forceinline__ float dot2(unsigned k, unsigned q, float acc) {
  return __builtin_amdgcn_fdot2_f32_bf16(__builtin_bit_cast(bf16x2, k), __builtin_bit_cast(bf16x2, q), acc, false);
}
__device__ __forceinline__ unsigned plo(unsigned r0, unsigned r1) { return __builtin_amdgcn_perm(r1, r0, 0x05040100u); }
__device__ __forceinline__ unsigned phi(unsigned r0, unsigned r1) { return __builtin_amdgcn_perm(r1, r0, 0x07060302u); }
template <int CTRL> __device__ __forceinline__ float dpp(float x) {
  return __builtin_bit_cast(float, __builtin_amdgcn_mov_dpp(__builtin_bit_cast(int, x), CTRL, 0xf, 0xf, true));
}
constexpr int XOR1 = 0xB1, XOR2 = 0x4E, HMIR = 0x141, MIR = 0x140, ROR8 = 0x128, ROR4 = 0x124;
__device__ __forceinline__ float sw32(float a, float b) {
  auto s = __builtin_amdgcn_permlane32_swap(__float_as_uint(a), __float_as_uint(b), false, false);
  return __uint_as_float(s[0]) + __uint_as_float(s[1]);
}
__device__ __forceinline__ float sw16(float a, float b) {
  auto s = __builtin_amdgcn_permlane16_swap(__float_as_uint(a), __float_as_uint(b), false, false);
  return __uint_as_float(s[0]) + __uint_as_float(s[1]);
}
__device__ __forceinline__ float xrow_max(float x) {
  auto s = __builtin_amdgcn_permlane16_swap(__float_as_uint(x), __float_as_uint(x), false, false);
  x = fmaxf(__uint_as_float(s[0]), __uint_as_float(s[1]));
  auto t = __builtin_amdgcn_permlane32_swap(__float_as_uint(x), __float_as_uint(x), false, false);
  return fmaxf(__uint_as_float(t[0]), __uint_as_float(t[1]));
}
__device__ __forceinline__ float grp8_sum(float x) { x += dpp<XOR1>(x); x += dpp<XOR2>(x); x += dpp<HMIR>(x); return x; }
__device__ __forceinline__ void lds_barrier() { asm volatile("s_waitcnt lgkmcnt(0)" ::: "memory"); __builtin_amdgcn_s_barrier(); asm volatile("" ::: "memory"); }
__device__ __forceinline__ float rdlane(float x, int l) { return __builtin_bit_cast(float, __builtin_amdgcn_readlane(__builtin_bit_cast(int, x), l)); }

__device__ __forceinline__ f32x2 wave_sum2(float a, float b) {
  float t = sw32(a, b);
  t = sw16(t, t);
  t = grp8_sum(t);
  t += dpp<MIR>(t);
  return f32x2{rdlane(t, 0), rdlane(t, 32)};
}
__device__ __forceinline__ void ln_stats2(const unsigned (&r)[2][5], float (&mb)[2], float (&rd)[2]) {
  float s[2] = {0.f, 0.f};
#pragma unroll
  for (int i = 0; i < 5; ++i) { s[0] = dot2(r[0][i], 0x3f803f80u, s[0]); s[1] = dot2(r[1][i], 0x3f803f80u, s[1]); }
  const f32x2 mean = wave_sum2(s[0], s[1]) * (1.0f / DM);
  f32x2 q[2] = {{0.f, 0.f}, {0.f, 0.f}};
#pragma unroll
  for (int i = 0; i < 5; ++i)
#pragma unroll
    for (int bb = 0; bb < 2; ++bb) { const f32x2 d = f32x2{bflo(r[bb][i]), bfhi(r[bb][i])} - mean[bb]; q[bb] = __builtin_elementwise_fma(d, d, q[bb]); }
  const f32x2 var = wave_sum2(q[0][0] + q[0][1], q[1][0] + q[1][1]) * (1.0f / DM);
#pragma unroll
  for (int bb = 0; bb < 2; ++bb) {
    mb[bb] = rbf(mean[bb]);
    rd[bb] = __builtin_amdgcn_rcpf(rbf(__builtin_amdgcn_sqrtf(rbf(rbf(var[bb]) + 1e-5f))));
  }
}
__device__ __forceinline__ unsigned ln_pair(unsigned v, float mb, float rd) {
  const unsigned d = pack2(bflo(v) - mb, bfhi(v) - mb);
  return pack2(bflo(d) * rd, bfhi(d) * rd);
}
__device__ __forceinline__ unsigned ln_one(unsigned v, float mb, float rd) { return pack2(rbf(bf1(v) - mb) * rd, 0.f); }

__device__ __forceinline__ void dma_half(unsigned lds, const char* gb, unsigned voff) {
  unsigned keep;
  asm volatile(
      "s_mov_b32 %0, m0\n\t"
      "s_mov_b32 m0, %3\n\t"
      "s_nop 0\n\t"
      "global_load_lds_dwordx4 %1, %2 nt\n\t"
      "s_add_u32 m0, m0, 0x380\n\t"
      "s_nop 0\n\t"
      "global_load_lds_dwordx4 %1, %2 offset:128 nt\n\t"
      "s_add_u32 m0, m0, 0x380\n\t"
      "s_nop 0\n\t"
      "global_load_lds_dwordx4 %1, %2 offset:256 nt\n\t"
      "s_mov_b32 m0, %0"
      : "=&s"(keep) : "v"(voff), "s"(gb), "s"(lds) : "memory", "scc");
}
__device__ __forceinline__ void dma_step(unsigned lds, const char* kb, const char* vb, unsigned voff) {
  unsigned keep;
  asm volatile(
      "s_waitcnt lgkmcnt(0)\n\t"
      "s_mov_b32 %0, m0\n\t"
      "s_mov_b32 m0, %4\n\t"
      "s_nop 0\n\t"
      "global_load_lds_dwordx4 %1, %2 nt\n\t"
      "s_add_u32 m0, m0, 0x380\n\t"
      "s_nop 0\n\t"
      "global_load_lds_dwordx4 %1, %2 offset:128 nt\n\t"
      "s_add_u32 m0, m0, 0x380\n\t"
      "s_nop 0\n\t"
      "global_load_lds_dwordx4 %1, %2 offset:256 nt\n\t"
      "s_add_u32 m0, m0, 0x500\n\t"
      "s_nop 0\n\t"
      "global_load_lds_dwordx4 %1, %3 nt\n\t"
      "s_add_u32 m0, m0, 0x380\n\t"
      "s_nop 0\n\t"
      "global_load_lds_dwordx4 %1, %3 offset:128 nt\n\t"
      "s_add_u32 m0, m0, 0x380\n\t"
      "s_nop 0\n\t"
      "global_load_lds_dwordx4 %1, %3 offset:256 nt\n\t"
      "s_mov_b32 m0, %0"
      : "=&s"(keep) : "v"(voff), "s"(kb), "s"(vb), "s"(lds) : "memory", "scc");
}
#define LD4NT(dst, voff, base, off) asm volatile("global_load_dwordx4 %0, %1, %2 offset:%c3 nt" : "=v"(dst) : "v"(voff), "s"(base), "i"(off) : "memory")
#define LD4NTP(dst, voff, base, off) asm volatile("global_load_dwordx4 %0, %1, %2 offset:%c3 nt" : "+v"(dst) : "v"(voff), "s"(base), "i"(off) : "memory")
#define LD1NT(dst, voff, base, off) asm volatile("global_load_dword %0, %1, %2 offset:%c3 nt" : "=v"(dst) : "v"(voff), "s"(base), "i"(off) : "memory")
#define LD4(dst, voff, base, off) asm volatile("global_load_dwordx4 %0, %1, %2 offset:%c3" : "=v"(dst) : "v"(voff), "s"(base), "i"(off) : "memory")
#define LD2(dst, voff, base, off) asm volatile("global_load_dwordx2 %0, %1, %2 offset:%c3" : "=v"(dst) : "v"(voff), "s"(base), "i"(off) : "memory")
#define LD1(dst, voff, base, off) asm volatile("global_load_dword %0, %1, %2 offset:%c3" : "=v"(dst) : "v"(voff), "s"(base), "i"(off) : "memory")
#define LDU16(dst, voff, base, off) asm volatile("global_load_ushort %0, %1, %2 offset:%c3" : "=v"(dst) : "v"(voff), "s"(base), "i"(off) : "memory")
#define WAITVM(n) asm volatile("s_waitcnt vmcnt(" #n ")" ::: "memory")
#define WAITVMX(n) WAITVM(n)

__device__ __forceinline__ void put_unit(__amdgpu_buffer_rsrc_t rs, unsigned off, unsigned v0, unsigned v1, unsigned tA, unsigned tB) {
  u32x4 u = {tA ^ v1, v0, tB ^ v0, v1};
  __builtin_amdgcn_raw_buffer_store_b128(u, rs, off, 0, 16);
}
__device__ __forceinline__ bool unit_ok(const u32x4& v, unsigned tA, unsigned tB) { return ((v[0] ^ v[3]) == tA) && ((v[2] ^ v[1]) == tB); }
template <int N>
__device__ __forceinline__ void get_units(__amdgpu_buffer_rsrc_t rs, const unsigned (&off)[N], u32x4 (&v)[N], bool active, unsigned tA, unsigned tB) {
  bool ok = !active;
#pragma nounroll
  for (unsigned spin = 0; spin < SPIN_CAP; ++spin) {
    if (!ok) {
      asm volatile("" ::: "memory");
#pragma unroll
      for (int k = 0; k < N; ++k) v[k] = __builtin_amdgcn_raw_buffer_load_b128(rs, off[k], 0, 16);
      bool g = true;
#pragma unroll
      for (int k = 0; k < N; ++k) g = g && unit_ok(v[k], tA, tB);
      ok = g;
    }
    if (__all(ok)) break;
    __builtin_amdgcn_s_sleep(1);
  }
}
template <int N>
__device__ __forceinline__ void get_units_sel(__amdgpu_buffer_rsrc_t rs, const unsigned (&off)[N], u32x4 (&v)[N], unsigned tA, unsigned tB) {
  bool ok[N];
#pragma unroll
  for (int k = 0; k < N; ++k) ok[k] = false;
#pragma nounroll
  for (unsigned spin = 0; spin < SPIN_CAP; ++spin) {
    asm volatile("" ::: "memory");
#pragma unroll
    for (int k = 0; k < N; ++k) if (!ok[k]) v[k] = __builtin_amdgcn_raw_buffer_load_b128(rs, off[k], 0, 16);
    bool g = true;
#pragma unroll
    for (int k = 0; k < N; ++k) { ok[k] = unit_ok(v[k], tA, tB); g = g && ok[k]; }
    if (__all(g)) break;
    __builtin_amdgcn_s_sleep(1);
  }
}
__device__ __forceinline__ f32x4 red16_g4(const float (&a)[16]) {
  float t[8], u[4];
#pragma unroll
  for (int k = 0; k < 8; ++k) t[k] = sw32(a[k], a[8 + k]);
#pragma unroll
  for (int k = 0; k < 4; ++k) { u[k] = sw16(t[k], t[k + 4]); u[k] += dpp<ROR8>(u[k]); u[k] += dpp<ROR4>(u[k]); }
  return f32x4{u[0], u[1], u[2], u[3]};
}

__global__ __launch_bounds__(NTHR) void fused_kernel(const u16* __restrict__ x, const u16* __restrict__ Kc, const u16* __restrict__ Vc, const u16* __restrict__ wq,
                                                      const u16* __restrict__ wo, const u16* __restrict__ w1, const u16* __restrict__ b1, const u16* __restrict__ w2,
                                                      const u16* __restrict__ b2, u16* __restrict__ out, unsigned char* __restrict__ ws) {
  __shared__ __attribute__((aligned(16))) unsigned char lds[L_END];
  const int bid = blockIdx.x, loc = bid >> 3;
  const int h = (bid & 7) * 4 + (loc >> 3), sx = loc & 7, b = sx >> 2, j = sx & 3, g = b * NH + h;
  const int tid = threadIdx.x, lane = tid & 63, wave = __builtin_amdgcn_readfirstlane(tid >> 6), r = lane >> 3, c = lane & 7;
  const unsigned lds0 = __builtin_amdgcn_readfirstlane((unsigned)(uintptr_t)(__attribute__((address_space(3))) unsigned char*)lds);
  const __amdgpu_buffer_rsrc_t rs = __builtin_amdgcn_make_buffer_rsrc(ws, 0, W_END, 0x00020000);
  float* qp = reinterpret_cast<float*>(lds + L_QP);
  unsigned* ql = reinterpret_cast<unsigned*>(lds + L_Q);
  unsigned* misc = reinterpret_cast<unsigned*>(lds + L_MISC);
  float* t1 = reinterpret_cast<float*>(lds + L_T1);
  unsigned* t2 = reinterpret_cast<unsigned*>(lds + L_T2);
#define STAMP(i)
  STAMP(0);
  const bool is_mlp = j == 0, is_op = j != 0 && sx != 7, is_red = is_op && wave == RW;
  const int jb = g, oi = sx - 1 - b, rid = h * 5 + oi;
  constexpr int nstep = 256;
  const char* kblk = reinterpret_cast<const char*>(Kc) + ((size_t)g * SK + (size_t)j * 8) * 384;
  const char* vblk = reinterpret_cast<const char*>(Vc) + ((size_t)g * SK + (size_t)j * 8) * 384;
  const int rot = g * ROTG;
  auto so = [&](int n) { return (size_t)((n + rot) & 255) * 12288; };
  const unsigned ring = lds0 + L_RING + wave * RINGW;
  const unsigned kvoff = r * 384 + c * 16;
  const int g4 = lane >> 2, c4 = lane & 3, rg = lane >> 4, c16 = lane & 15;
  u32x2 ep;
  if (wave == NW - 1) {
    asm volatile("global_load_dwordx2 %0, %1, %2 sc1" : "=v"(ep) : "v"(0u), "s"(ws) : "memory");
    if (lane == 0) { misc[5] = 0; misc[6] = 0; }
  }
  const int qi = (0x2F41F530 >> (4 * sx)) & 15;
  if (qi < 6) {
    u32x4 xa[2], wv[5];
    unsigned xb[2], xs[2];
    u32x2 xo[2];
    const int k0 = (qi >= 3 ? 320 : 0) + 40 * wave;
    const unsigned o16 = lane * 16, o4 = lane * 4, oo = (k0 + 4 * r) * 2, os = (k0 + 32 + r) * 2;
    LD4(xa[0], o16, x, 0); LD1(xb[0], o4, x, 1024); LD4(xa[1], o16, x, 1280); LD1(xb[1], o4, x, 2304);
    LD2(xo[0], oo, x, 0); LDU16(xs[0], os, x, 0); LD2(xo[1], oo, x, 1280); LDU16(xs[1], os, x, 1280);
    const unsigned wcol = (unsigned)(h * 384 + (qi >= 3 ? qi - 3 : qi) * 128 + c * 16);
    const unsigned wa = (unsigned)(k0 + 4 * r) * 12288u + wcol, wb = (unsigned)(k0 + 32 + r) * 12288u + wcol;
    const char* wqc = reinterpret_cast<const char*>(wq);
    LD4NT(wv[0], wa, wqc, 0); LD4NT(wv[1], wa, wqc + 12288, 0); LD4NT(wv[2], wa, wqc + 24576, 0); LD4NT(wv[3], wa, wqc + 36864, 0); LD4NT(wv[4], wb, wqc, 0);
    dma_half(ring, kblk + so(wave), kvoff);
    dma_half(ring + 3072, vblk + so(wave), kvoff);
    asm volatile("s_waitcnt vmcnt(11) ; XIN_LANDED %0 %1 %2 %3 %4 %5 %6 %7" : "+v"(xa[0]), "+v"(xa[1]), "+v"(xb[0]), "+v"(xb[1]), "+v"(xo[0]), "+v"(xo[1]), "+v"(xs[0]), "+v"(xs[1])::"memory");
    const unsigned xr[2][5] = {{xa[0][0], xa[0][1], xa[0][2], xa[0][3], xb[0]}, {xa[1][0], xa[1][1], xa[1][2], xa[1][3], xb[1]}};
    float mb[2], rd[2];
    ln_stats2(xr, mb, rd);
    unsigned xp[2][3];
#pragma unroll
    for (int bb = 0; bb < 2; ++bb) { xp[bb][0] = ln_pair(xo[bb][0], mb[bb], rd[bb]); xp[bb][1] = ln_pair(xo[bb][1], mb[bb], rd[bb]); xp[bb][2] = ln_one(xs[bb], mb[bb], rd[bb]); }
    asm volatile("s_waitcnt vmcnt(6) ; WQ_LANDED %0 %1 %2 %3 %4" : "+v"(wv[0]), "+v"(wv[1]), "+v"(wv[2]), "+v"(wv[3]), "+v"(wv[4]), "+v"(xp[0][0]), "+v"(xp[0][1]), "+v"(xp[0][2]), "+v"(xp[1][0]), "+v"(xp[1][1]), "+v"(xp[1][2])::"memory");
    STAMP(1);
    float a[16];
#pragma unroll
    for (int k = 0; k < 4; ++k) {
      const unsigned p0 = plo(wv[0][k], wv[1][k]), p1 = phi(wv[0][k], wv[1][k]), p2 = plo(wv[2][k], wv[3][k]), p3 = phi(wv[2][k], wv[3][k]);
      const unsigned p4 = wv[4][k] & 0xffffu, p5 = wv[4][k] >> 16;
#pragma unroll
      for (int bb = 0; bb < 2; ++bb) {
        a[bb * 8 + 2 * k] = dot2(p4, xp[bb][2], dot2(p2, xp[bb][1], dot2(p0, xp[bb][0], 0.f)));
        a[bb * 8 + 2 * k + 1] = dot2(p5, xp[bb][2], dot2(p3, xp[bb][1], dot2(p1, xp[bb][0], 0.f)));
      }
    }
    float t[8], u[4];
#pragma unroll
    for (int k = 0; k < 8; ++k) t[k] = sw32(a[k], a[8 + k]);
#pragma unroll
    for (int k = 0; k < 4; ++k) { u[k] = sw16(t[k], t[k + 4]); u[k] += dpp<ROR8>(u[k]); }
    if ((lane & 8) == 0) *reinterpret_cast<f32x4*>(t1 + wave * 128 + (lane >> 5) * 64 + c * 8 + ((lane >> 4) & 1) * 4) = f32x4{u[0], u[1], u[2], u[3]};
  } else {
    dma_half(ring, kblk + so(wave), kvoff);
    dma_half(ring + 3072, vblk + so(wave), kvoff);
    if (wave == NW - 1) WAITVM(6);
  }
  lds_barrier();
  STAMP(2);
  if (wave == NW - 1) {
    asm volatile("; EP_LANDED %0" : "+v"(ep));
    const u64 e1 = (((u64)ep[1] << 32) | ep[0]) + 1ull;
    const unsigned tA = (unsigned)e1, tB = (((unsigned)e1 * 0x9E3779B1u) ^ (unsigned)(e1 >> 32)) | 1u;
    if (qi < 6) {
      f32x2 a = {0.f, 0.f};
#pragma unroll
      for (int w = 0; w < NW; ++w) a += *reinterpret_cast<const f32x2*>(t1 + w * 128 + lane * 2);
      put_unit(rs, W_QX + (unsigned)((h * 6 + qi) * 64 + lane) * 16, __float_as_uint(a[0]), __float_as_uint(a[1]), tA, tB);
    }
    if (lane == 0) { misc[0] = (NSLOT + 3 + XSTEP) * NW; misc[1] = tA; misc[2] = tB; misc[3] = (unsigned)e1; misc[4] = (unsigned)(e1 >> 32); }
    unsigned off[3];
#pragma unroll
    for (int k = 0; k < 3; ++k) off[k] = W_QX + (unsigned)((h * 6 + (lane >> 5) * 3 + k) * 64 + b * 32 + (lane & 31)) * 16;
    u32x4 v[3];
    get_units_sel<3>(rs, off, v, tA, tB);
#pragma unroll
    for (int k = 0; k < 3; ++k) reinterpret_cast<u16*>(ql)[(k * 32 + (lane & 31)) * 2 + (lane >> 5)] = (u16)(pack2(sw32(__uint_as_float(v[k][1]), __uint_as_float(v[k][3])), 0.f) & 0xffffu);
    asm volatile("s_waitcnt lgkmcnt(0)" ::: "memory");
    if (lane == 0) __hip_atomic_store(misc + 5, 1u, __ATOMIC_RELAXED, __HIP_MEMORY_SCOPE_WORKGROUP);
    STAMP(9);
  }
  u32x4 wov[6];
  unsigned bsm = 0, rx = 0, rb2 = 0;
  const char* w1c = reinterpret_cast<const char*>(w1) + jb * 48;
  const char* w2c = reinterpret_cast<const char*>(w2) + (size_t)jb * 30720;
  {
    if (is_mlp) {
      const unsigned bo = (unsigned)(jb * 24 + (lane < 48 ? lane % 24 : 0)) * 2;
      LDU16(bsm, bo, b1, 0);
      LD1(rx, (unsigned)(lane & 1) * (639u * 3072u), w1c, 0);
      LD1(rb2, (unsigned)(lane & 1) * 30716u, w2c, 0);
    } else if (is_red) {
      const unsigned col = (unsigned)((rid % 80) * 8 + 2 * c4 + (lane >> 5));
      LDU16(rx, col * 2, x + (rid / 80) * DM, 0);
      LDU16(rb2, col * 2, b2, 0);
    }
    WAITVMX(PD); dma_half(ring + 6144, kblk + so(wave + 8), kvoff);
    WAITVMX(PD); dma_half(ring + 9216, vblk + so(wave + 8), kvoff);
    WAITVMX(PD); dma_half(ring + 12288, kblk + so(wave + 16), kvoff);
    WAITVMX(PD); dma_half(ring + 15360, vblk + so(wave + 16), kvoff);
    if (is_op) {
      const char* woc = reinterpret_cast<const char*>(wo) + (size_t)(h * DH + 24 * wave) * 1280 + oi * 256;
      const unsigned oa = (unsigned)(4 * rg) * 1280u + c16 * 16, ob = (unsigned)(16 + 2 * rg) * 1280u + c16 * 16;
      WAITVMX(PD); LD4NT(wov[0], oa, woc, 0); LD4NT(wov[1], oa, woc, 1280); LD4NT(wov[2], oa, woc, 2560);
      WAITVMX(PD); LD4NT(wov[3], oa, woc, 3840); LD4NT(wov[4], ob, woc, 0); LD4NT(wov[5], ob, woc, 1280);
    }
  }
  while (__hip_atomic_load(misc + 5, __ATOMIC_RELAXED, __HIP_MEMORY_SCOPE_WORKGROUP) == 0) __builtin_amdgcn_s_sleep(1);
  asm volatile("" ::: "memory");
  STAMP(3);
  const unsigned tA = misc[1], tB = misc[2];
  float m = -INFINITY, l = 0.f;
  float acc[24];
  unsigned wop[24];
  {
    u32x4 q[3];
#pragma unroll
    for (int t = 0; t < 3; ++t) q[t] = *reinterpret_cast<const u32x4*>(ql + t * 32 + c * 4);
#pragma unroll
    for (int k = 0; k < 24; ++k) acc[k] = 0.f;
    constexpr float SC = 0.07216878364870322f, L2E = 1.4426950408889634f;
    int nxt = (NSLOT + XSTEP) * NW + wave;
    int slot = 0, it = 0;
    const unsigned char* rp = lds + L_RING + wave * RINGW + lane * 16;
    auto step = [&](const u32x4 (&kb)[3], const u32x4 (&vb)[3]) {
      float d = 0.f;
#pragma unroll
      for (int t = 0; t < 3; ++t)
#pragma unroll
        for (int k = 0; k < 4; ++k) d = dot2(kb[t][k], q[t][k], d);
      d = grp8_sum(d);
      const float s_ = rbf(rbf(d) * SC) * L2E;
      if (__any(s_ > m)) {
        const float mx = fmaxf(m, s_), al = __builtin_amdgcn_exp2f(m - mx);
        l *= al;
#pragma unroll
        for (int k = 0; k < 24; ++k) acc[k] *= al;
        m = mx;
      }
      const float p = __builtin_amdgcn_exp2f(s_ - m);
      l += p;
#pragma unroll
      for (int t = 0; t < 3; ++t)
#pragma unroll
        for (int k = 0; k < 4; ++k) {
          acc[t * 8 + 2 * k] = fmaf(p, bflo(vb[t][k]), acc[t * 8 + 2 * k]);
          acc[t * 8 + 2 * k + 1] = fmaf(p, bfhi(vb[t][k]), acc[t * 8 + 2 * k + 1]);
        }
    };
#pragma nounroll
    for (; nxt < nstep; ++it) {
      asm volatile("s_waitcnt vmcnt(12)" ::: "memory");
      u32x4 kb[3], vb[3];
#pragma unroll
      for (int t = 0; t < 3; ++t) { kb[t] = *reinterpret_cast<const u32x4*>(rp + slot * 6144 + t * 1024); vb[t] = *reinterpret_cast<const u32x4*>(rp + slot * 6144 + 3072 + t * 1024); }
      dma_step(ring + slot * 6144, kblk + so(nxt), vblk + so(nxt), kvoff);
      if (it < 2) nxt += NW;
      else {
        int n2 = 0;
        if (lane == 0) n2 = __hip_atomic_fetch_add(misc, 1u, __ATOMIC_RELAXED, __HIP_MEMORY_SCOPE_WORKGROUP);
        nxt = __builtin_amdgcn_readfirstlane(n2);
      }
      step(kb, vb);
      if (it == 2) {
        asm volatile("s_waitcnt vmcnt(18) ; X_LANDED %0 %1 %2 %3 %4 %5 %6 %7 %8" : "+v"(bsm), "+v"(rx), "+v"(rb2), "+v"(wov[0]), "+v"(wov[1]), "+v"(wov[2]), "+v"(wov[3]), "+v"(wov[4]), "+v"(wov[5])::"memory");
#pragma unroll
        for (int pr = 0; pr < 3; ++pr)
#pragma unroll
          for (int k = 0; k < 4; ++k) { wop[pr * 8 + 2 * k] = plo(wov[2 * pr][k], wov[2 * pr + 1][k]); wop[pr * 8 + 2 * k + 1] = phi(wov[2 * pr][k], wov[2 * pr + 1][k]); }
      }
      slot = slot == NSLOT - 1 ? 0 : slot + 1;
    }
#pragma nounroll
    for (int wc = 2; wc >= 0; --wc) {
      if (wc == 2) asm volatile("s_waitcnt vmcnt(12)" ::: "memory");
      else if (wc == 1) asm volatile("s_waitcnt vmcnt(6)" ::: "memory");
      else asm volatile("s_waitcnt vmcnt(0)" ::: "memory");
      u32x4 kb[3], vb[3];
#pragma unroll
      for (int t = 0; t < 3; ++t) { kb[t] = *reinterpret_cast<const u32x4*>(rp + slot * 6144 + t * 1024); vb[t] = *reinterpret_cast<const u32x4*>(rp + slot * 6144 + 3072 + t * 1024); }
      step(kb, vb);
      slot = slot == NSLOT - 1 ? 0 : slot + 1;
    }
    STAMP(4);
    const float M = xrow_max(fmaxf(m, dpp<ROR8>(m)));
    const float al = __builtin_amdgcn_exp2f(m - M);
    l *= al; l += dpp<ROR8>(l); l = sw16(l, l); l = sw32(l, l);
    float t12[12], t6[6];
#pragma unroll
    for (int k = 0; k < 12; ++k) t12[k] = sw32(acc[k] * al, acc[k + 12] * al);
#pragma unroll
    for (int k = 0; k < 6; ++k) { t6[k] = sw16(t12[k], t12[k + 6]); t6[k] += dpp<ROR8>(t6[k]); }
    float* rec = reinterpret_cast<float*>(lds + L_RING + wave * RINGW);
    if ((lane & 8) == 0) {
      float* p = rec + 4 + c * 24 + 12 * (lane >> 5) + 6 * ((lane >> 4) & 1);
#pragma unroll
      for (int k = 0; k < 3; ++k) *reinterpret_cast<f32x2*>(p + 2 * k) = f32x2{t6[2 * k], t6[2 * k + 1]};
      if (lane == 0) *reinterpret_cast<f32x2*>(rec) = f32x2{M, l};
    }
  }
  lds_barrier();
  if (tid < 97) {
    const int d = 2 * tid, idx = tid < 96 ? 4 + ((d & 63) >> 3) * 24 + (d >> 6) * 8 + (d & 7) : 0;
    f32x2 ml[NW], a[NW];
#pragma unroll
    for (int w = 0; w < NW; ++w) {
      const float* rec = reinterpret_cast<const float*>(lds + L_RING + w * RINGW);
      ml[w] = *reinterpret_cast<const f32x2*>(rec); a[w] = *reinterpret_cast<const f32x2*>(rec + idx);
    }
    float M = ml[0][0];
#pragma unroll
    for (int w = 1; w < NW; ++w) M = fmaxf(M, ml[w][0]);
    f32x2 o = {0.f, 0.f};
#pragma unroll
    for (int w = 0; w < NW; ++w) { const float wt = __builtin_amdgcn_exp2f(ml[w][0] - M); o = __builtin_elementwise_fma(f32x2{wt, wt}, a[w], o); }
    put_unit(rs, W_P1 + (unsigned)(h * 8 + sx) * 1792 + tid * 16, __float_as_uint(tid < 96 ? o[0] : M), __float_as_uint(o[1]), tA, tB);
  }
  if (tid == 128) put_unit(rs, W_DN + (unsigned)bid * 16, 0u, 0u, tA, tB);
  if (is_op) {
  if (wave >= 5) {
    const int t3 = tid - 320, bb = t3 >= 96, u = t3 - 96 * bb;
    const unsigned base = W_P1 + (unsigned)(h * 8 + bb * 4) * 1792;
    u32x4 v[4], f[4];
#pragma unroll
    for (int jj = 0; jj < 4; ++jj) { f[jj] = __builtin_amdgcn_raw_buffer_load_b128(rs, base + jj * 1792 + 96 * 16, 0, 16); v[jj] = __builtin_amdgcn_raw_buffer_load_b128(rs, base + jj * 1792 + u * 16, 0, 16); }
#pragma nounroll
    for (unsigned spin = 0; spin < SPIN_CAP; ++spin) {
      bool okf[4], okv[4];
      int miss = 0;
      bool all = true;
#pragma unroll
      for (int jj = 0; jj < 4; ++jj) { okf[jj] = unit_ok(f[jj], tA, tB); okv[jj] = unit_ok(v[jj], tA, tB); miss += okf[jj] ? 0 : 1; all = all && okf[jj] && okv[jj]; }
      if (__all(all)) break;
      __builtin_amdgcn_s_sleep(1);
      asm volatile("" ::: "memory");
#pragma unroll
      for (int jj = 0; jj < 4; ++jj) {
        if (!okf[jj]) f[jj] = __builtin_amdgcn_raw_buffer_load_b128(rs, base + jj * 1792 + 96 * 16, 0, 16);
        if (!okv[jj] && (okf[jj] || miss <= 1)) v[jj] = __builtin_amdgcn_raw_buffer_load_b128(rs, base + jj * 1792 + u * 16, 0, 16);
      }
    }
    const float M = fmaxf(fmaxf(__uint_as_float(f[0][1]), __uint_as_float(f[1][1])), fmaxf(__uint_as_float(f[2][1]), __uint_as_float(f[3][1])));
    float L = 0.f, o0 = 0.f, o1 = 0.f;
#pragma unroll
    for (int jj = 0; jj < 4; ++jj) {
      const float wt = __builtin_amdgcn_exp2f(__uint_as_float(f[jj][1]) - M);
      L = fmaf(wt, __uint_as_float(f[jj][3]), L); o0 = fmaf(wt, __uint_as_float(v[jj][1]), o0); o1 = fmaf(wt, __uint_as_float(v[jj][3]), o1);
    }
    const float rl = __builtin_amdgcn_rcpf(L);
    t2[t3] = pack2(o0 * rl, o1 * rl);
  }
  lds_barrier();
  STAMP(5);
  {
    float a[16];
#pragma unroll
    for (int bb = 0; bb < 2; ++bb) {
      const u32x2 p01 = *reinterpret_cast<const u32x2*>(t2 + bb * 96 + 12 * wave + 2 * rg);
      const unsigned p2 = t2[bb * 96 + 12 * wave + 8 + rg];
#pragma unroll
      for (int k = 0; k < 8; ++k) a[bb * 8 + k] = dot2(wop[16 + k], p2, dot2(wop[8 + k], p01[1], dot2(wop[k], p01[0], 0.f)));
    }
    float t[8];
#pragma unroll
    for (int k = 0; k < 8; ++k) t[k] = sw32(a[k], a[8 + k]);
    const f32x4 s4 = {sw16(t[0], t[4]), sw16(t[1], t[5]), sw16(t[2], t[6]), sw16(t[3], t[7])};
    *reinterpret_cast<f32x4*>(t1 + wave * 256 + (lane >> 5) * 128 + c16 * 8 + ((lane >> 4) & 1) * 4) = s4;
  }
  lds_barrier();
  if (tid < 128) {
    f32x2 a = {0.f, 0.f};
#pragma unroll
    for (int w = 0; w < NW; ++w) a += *reinterpret_cast<const f32x2*>(t1 + w * 256 + 2 * tid);
    put_unit(rs, W_Y2 + (unsigned)(h * 5 + oi) * 2048 + tid * 16, __float_as_uint(a[0]), __float_as_uint(a[1]), tA, tB);
  }
  STAMP(6);
    if (is_red) {
      const int bq = rid / 80, cg = rid % 80;
      float hv;
      {
        unsigned off[2];
#pragma unroll
        for (int k = 0; k < 2; ++k) off[k] = W_Y2 + (unsigned)((16 * k + g4) * 5 + (cg >> 4)) * 2048 + (unsigned)(bq * 64 + (cg & 15) * 4 + c4) * 16;
        u32x4 v[2];
        get_units<2>(rs, off, v, true, tA, tB);
        const float a0 = __uint_as_float(v[0][1]) + __uint_as_float(v[1][1]);
        const float a1 = __uint_as_float(v[0][3]) + __uint_as_float(v[1][3]);
        float s = sw32(a0, a1); s = sw16(s, s); s += dpp<ROR8>(s); s += dpp<ROR4>(s);
        hv = rbf(bf1(rx) + rbf(s));
        auto e = __builtin_amdgcn_permlane32_swap(__float_as_uint(hv), __float_as_uint(hv), false, false);
        const unsigned pk = (e[0] >> 16) | (e[1] & 0xffff0000u);
        const unsigned pn = (unsigned)__builtin_amdgcn_mov_dpp((int)pk, XOR1, 0xf, 0xf, true);
        if (lane < 4 && !(lane & 1)) put_unit(rs, W_H3 + (unsigned)(rid * 2 + (lane >> 1)) * 16, pk, pn, tA, tB);
      }
      STAMP(7);
      {
        unsigned off[4];
#pragma unroll
        for (int k = 0; k < 4; ++k) off[k] = W_O4 + (unsigned)(16 * k + g4) * 10240 + (unsigned)(bq * 320 + cg * 4 + c4) * 16;
        u32x4 v[4];
        get_units<4>(rs, off, v, true, tA, tB);
        const float a0 = (__uint_as_float(v[0][1]) + __uint_as_float(v[1][1])) + (__uint_as_float(v[2][1]) + __uint_as_float(v[3][1]));
        const float a1 = (__uint_as_float(v[0][3]) + __uint_as_float(v[1][3])) + (__uint_as_float(v[2][3]) + __uint_as_float(v[3][3]));
        float s = sw32(a0, a1); s = sw16(s, s); s += dpp<ROR8>(s); s += dpp<ROR4>(s);
        const float ov = rbf(hv + rbf(rbf(s) + bf1(rb2)));
        auto e = __builtin_amdgcn_permlane32_swap(__float_as_uint(ov), __float_as_uint(ov), false, false);
        if (lane < 4) reinterpret_cast<unsigned*>(out)[bq * 320 + cg * 4 + lane] = (e[0] >> 16) | (e[1] & 0xffff0000u);
        if (rid == 0 && lane == 0) __hip_atomic_store(reinterpret_cast<u64*>(ws + W_EPOCH), ((u64)misc[4] << 32) | misc[3], __ATOMIC_RELAXED, __HIP_MEMORY_SCOPE_AGENT);
      }
    }
  } else if (is_mlp) {
    const int li = lane < 48 ? lane : 0, lb = li >= 24;
    float csum = 0.f;
    unsigned w1p[24], w2v[24];
    {
      if (LATE) {
        if (wave == 7) {
          const unsigned smo = W_DN + (unsigned)((bid & 192) + lane) * 16;
#pragma nounroll
          for (unsigned spin = 0; spin < SPIN_CAP; ++spin) {
            const u32x4 v = __builtin_amdgcn_raw_buffer_load_b128(rs, smo, 0, 16);
            if (__builtin_popcountll(__ballot(unit_ok(v, tA, tB))) >= THR) break;
            __builtin_amdgcn_s_sleep(4);
          }
          if (lane == 0) __hip_atomic_store(misc + 6, 1u, __ATOMIC_RELAXED, __HIP_MEMORY_SCOPE_WORKGROUP);
        } else {
          while (__hip_atomic_load(misc + 6, __ATOMIC_RELAXED, __HIP_MEMORY_SCOPE_WORKGROUP) == 0) __builtin_amdgcn_s_sleep(1);
        }
      }
      u32x4 w1v[5];
      const unsigned w1a = (unsigned)(80 * wave + 4 * g4) * 3072u + (c4 < 3 ? c4 : 2) * 16, w1b = (unsigned)(80 * wave + 64 + g4) * 3072u + (c4 < 3 ? c4 : 2) * 16;
      LD4NT(w1v[0], w1a, w1c, 0); LD4NT(w1v[1], w1a, w1c, 3072); LD4NT(w1v[2], w1a, w1c + 6144, 0); LD4NT(w1v[3], w1a, w1c + 6144, 3072); LD4NT(w1v[4], w1b, w1c, 0);
      const unsigned o2 = (unsigned)(tid < 320 ? tid : 0) * 4;
      if (wave < 5) {
#pragma unroll
        for (int k = 0; k < 24; k += 3) { LD1NT(w2v[k], o2, w2c + k * 1280, 0); LD1NT(w2v[k + 1], o2, w2c + k * 1280, 1280); LD1NT(w2v[k + 2], o2, w2c + k * 1280, 2560); }
        asm volatile("s_waitcnt vmcnt(24)" ::: "memory");
      } else {
        asm volatile("s_waitcnt vmcnt(0)" ::: "memory");
      }
      asm volatile("; W1_LANDED %0 %1 %2 %3 %4" : "+v"(w1v[0]), "+v"(w1v[1]), "+v"(w1v[2]), "+v"(w1v[3]), "+v"(w1v[4])::"memory");
      STAMP(5);
#pragma unroll
      for (int k = 0; k < 4; ++k) {
        w1p[2 * k] = plo(w1v[0][k], w1v[1][k]); w1p[2 * k + 1] = phi(w1v[0][k], w1v[1][k]);
        w1p[8 + 2 * k] = plo(w1v[2][k], w1v[3][k]); w1p[8 + 2 * k + 1] = phi(w1v[2][k], w1v[3][k]);
        w1p[16 + 2 * k] = w1v[4][k] & 0xffffu; w1p[16 + 2 * k + 1] = w1v[4][k] >> 16;
      }
      float cs[8];
#pragma unroll
      for (int k = 0; k < 8; ++k) cs[k] = dot2(w1p[16 + k], 0x3f803f80u, dot2(w1p[8 + k], 0x3f803f80u, dot2(w1p[k], 0x3f803f80u, 0.f)));
      float c0 = sw16(sw32(cs[0], cs[4]), sw32(cs[2], cs[6])), c1 = sw16(sw32(cs[1], cs[5]), sw32(cs[3], cs[7]));
      c0 += dpp<ROR8>(c0); c0 += dpp<ROR4>(c0); c1 += dpp<ROR8>(c1); c1 += dpp<ROR4>(c1);
      if ((lane & 12) == 0 && c4 < 3) *reinterpret_cast<f32x2*>(lds + L_CS + (wave * 24 + c4 * 8 + 4 * (lane >> 5) + 2 * ((lane >> 4) & 1)) * 4) = f32x2{c0, c1};
      asm volatile("s_waitcnt vmcnt(0) ; W2_LANDED %0 %1 %2 %3 %4 %5 %6 %7 %8 %9 %10 %11 %12 %13 %14 %15 %16 %17 %18 %19 %20 %21 %22 %23" : "+v"(w2v[0]), "+v"(w2v[1]), "+v"(w2v[2]), "+v"(w2v[3]), "+v"(w2v[4]), "+v"(w2v[5]), "+v"(w2v[6]), "+v"(w2v[7]), "+v"(w2v[8]), "+v"(w2v[9]), "+v"(w2v[10]), "+v"(w2v[11]),
                   "+v"(w2v[12]), "+v"(w2v[13]), "+v"(w2v[14]), "+v"(w2v[15]), "+v"(w2v[16]), "+v"(w2v[17]), "+v"(w2v[18]), "+v"(w2v[19]), "+v"(w2v[20]), "+v"(w2v[21]), "+v"(w2v[22]), "+v"(w2v[23]));
      STAMP(6);
#pragma unroll
      for (int i = 0; i < 12; ++i) { const unsigned lo = plo(w2v[2 * i], w2v[2 * i + 1]), hi = phi(w2v[2 * i], w2v[2 * i + 1]); w2v[2 * i] = lo; w2v[2 * i + 1] = hi; }
      lds_barrier();
      if (wave < 5) {
#pragma unroll
        for (int w = 0; w < NW; ++w) csum += reinterpret_cast<const float*>(lds + L_CS)[w * 24 + li - 24 * lb];
      }
      asm volatile("" : "+v"(csum), "+v"(w2v[0]), "+v"(w2v[1]), "+v"(w2v[2]), "+v"(w2v[3]), "+v"(w2v[4]), "+v"(w2v[5]), "+v"(w2v[6]), "+v"(w2v[7]), "+v"(w2v[8]), "+v"(w2v[9]), "+v"(w2v[10]), "+v"(w2v[11]),
                   "+v"(w2v[12]), "+v"(w2v[13]), "+v"(w2v[14]), "+v"(w2v[15]), "+v"(w2v[16]), "+v"(w2v[17]), "+v"(w2v[18]), "+v"(w2v[19]), "+v"(w2v[20]), "+v"(w2v[21]), "+v"(w2v[22]), "+v"(w2v[23]));
    }
    if (wave < 5) {
      unsigned off[1] = {W_H3 + (unsigned)tid * 16};
      u32x4 v[1];
      get_units<1>(rs, off, v, true, tA, tB);
      *reinterpret_cast<u32x2*>(t2 + 2 * tid) = u32x2{v[0][1], v[0][3]};
      float s = dot2(v[0][1], 0x3f803f80u, dot2(v[0][3], 0x3f803f80u, 0.f));
      s = grp8_sum(s); s += dpp<MIR>(s); s = sw16(s, s);
      const float mu = s * (1.0f / 128.f);
      const f32x2 d0 = f32x2{bflo(v[0][1]), bfhi(v[0][1])} - mu, d1 = f32x2{bflo(v[0][3]), bfhi(v[0][3])} - mu;
      const f32x2 q2 = __builtin_elementwise_fma(d0, d0, d1 * d1);
      float q = q2[0] + q2[1];
      q = grp8_sum(q); q += dpp<MIR>(q); q = sw16(q, q);
      if ((lane & 31) == 0) *reinterpret_cast<f32x2*>(lds + L_ST + (tid >> 5) * 8) = f32x2{mu, q};
    }
    lds_barrier();
    STAMP(7);
    {
      float a[16];
#pragma unroll
      for (int bb = 0; bb < 2; ++bb) {
        const u32x2 ho = *reinterpret_cast<const u32x2*>(t2 + bb * 320 + 40 * wave + 2 * g4);
        const unsigned hs = reinterpret_cast<const u16*>(t2)[bb * 640 + 80 * wave + 64 + g4];
#pragma unroll
        for (int k = 0; k < 8; ++k) a[bb * 8 + k] = dot2(w1p[16 + k], hs, dot2(w1p[8 + k], ho[1], dot2(w1p[k], ho[0], 0.f)));
      }
      const f32x4 s4 = red16_g4(a);
      if ((lane & 12) == 0 && c4 < 3) *reinterpret_cast<f32x4*>(qp + wave * 48 + (lane >> 5) * 24 + c4 * 8 + ((lane >> 4) & 1) * 4) = s4;
    }
    float mb = 0.f, rd = 0.f;
    if (wave < 5) {
      f32x2 sg[5];
#pragma unroll
      for (int k = 0; k < 5; ++k) sg[k] = *reinterpret_cast<const f32x2*>(lds + L_ST + (lb * 5 + k) * 8);
      const float mean = ((sg[0][0] + sg[1][0]) + (sg[2][0] + sg[3][0]) + sg[4][0]) * 0.2f;
      float m2 = (sg[0][1] + sg[1][1]) + (sg[2][1] + sg[3][1]) + sg[4][1], dd = 0.f;
#pragma unroll
      for (int k = 0; k < 5; ++k) { const float d = sg[k][0] - mean; dd = fmaf(d, d, dd); }
      const float var = fmaf(dd, 128.f, m2) * (1.0f / DM);
      mb = rbf(mean);
      rd = __builtin_amdgcn_rcpf(rbf(__builtin_amdgcn_sqrtf(rbf(rbf(var) + 1e-5f))));
    }
    lds_barrier();
    if (wave < 5) {
      float s = 0.f;
#pragma unroll
      for (int w = 0; w < NW; ++w) s += qp[w * 48 + li];
      const float hv = rbf(rbf(fmaf(-mb, csum, s) * rd) + bf1(bsm));
      const float rr = hv > 0.f ? hv : 0.f;
      const float r2 = rr * rr;
      const unsigned rp2 = pack2(r2, dpp<XOR1>(r2));
      float a00 = 0.f, a01 = 0.f, a10 = 0.f, a11 = 0.f;
#pragma unroll
      for (int i = 0; i < 12; ++i) {
        const unsigned p0 = (unsigned)__builtin_amdgcn_readlane((int)rp2, 2 * i), p1 = (unsigned)__builtin_amdgcn_readlane((int)rp2, 24 + 2 * i);
        a00 = dot2(w2v[2 * i], p0, a00); a01 = dot2(w2v[2 * i + 1], p0, a01); a10 = dot2(w2v[2 * i], p1, a10); a11 = dot2(w2v[2 * i + 1], p1, a11);
      }
      put_unit(rs, W_O4 + (unsigned)jb * 10240 + (unsigned)tid * 16, __float_as_uint(a00), __float_as_uint(a01), tA, tB);
      put_unit(rs, W_O4 + (unsigned)jb * 10240 + (unsigned)(320 + tid) * 16, __float_as_uint(a10), __float_as_uint(a11), tA, tB);
    }
  }
  STAMP(8);
}

extern "C" void kernel_launch(void* const* d_in, const int* in_sizes, int n_in, void* d_out, int out_size, void* d_ws, size_t ws_size, hipStream_t stream) {
  fused_kernel<<<256, NTHR, 0, stream>>>((const u16*)d_in[0], (const u16*)d_in[1], (const u16*)d_in[2], (const u16*)d_in[3], (const u16*)d_in[4], (const u16*)d_in[6],
                                         (const u16*)d_in[7], (const u16*)d_in[8], (const u16*)d_in[9], (u16*)d_out, (unsigned char*)d_ws);
}
```

```cpp
#include <hip/hip_runtime.h>
#include <hip/hip_bf16.h>
#include <stdint.h>

typedef unsigned u32x4 __attribute__((ext_vector_type(4)));
typedef unsigned u32x2 __attribute__((ext_vector_type(2)));
typedef float f32x4 __attribute__((ext_vector_type(4)));
typedef float f32x2 __attribute__((ext_vector_type(2)));
typedef __bf16 bf16x2 __attribute__((ext_vector_type(2)));
typedef short s16x4 __attribute__((ext_vector_type(4)));
typedef __bf16 bf16x8 __attribute__((ext_vector_type(8)));
typedef unsigned short u16;
typedef unsigned long long u64;

#define STAMPS 0
#define PD 3

#define ROTG 5
#define THR 40
#define RW 7
#define H3R 16
#define H3S 5120
constexpr int DM = 640, NH = 32, SK = 8192, DH = 192, FF = 1536;
constexpr int NW = 8;
constexpr int NTHR = NW * 64;
constexpr int NRED = 160, NMLP = 64;
constexpr int RINGW = 18432;
constexpr int L_RING = 0;
constexpr int L_QP = NW * RINGW;
constexpr int L_Q = L_QP + 1536;
constexpr int L_MISC = L_Q + 384;
constexpr int L_T1 = L_MISC + 64;
constexpr int L_T2 = L_T1 + 8192;
constexpr int L_CS = L_T2 + 2560;
constexpr int L_ST = L_CS + 768;
constexpr int L_END = L_ST + 128;
static_assert(L_END <= 163840, "lds");
constexpr unsigned W_EPOCH = 0;
constexpr unsigned W_DN = 4096;
constexpr unsigned W_QX = 8192;
constexpr unsigned W_P1 = 262144;
constexpr unsigned W_Y2 = W_P1 + 256 * 1792;
constexpr unsigned W_H3 = W_Y2 + 256 * 1280;
constexpr unsigned W_O4 = W_H3 + 32 * 8192;
constexpr unsigned W_END = W_O4 + 64 * 10240;
constexpr unsigned W_STAMP = 4u << 20;
constexpr unsigned SPIN_CAP = 400000;
constexpr int NSTAMP = 10;

__device__ __forceinline__ float rbf(float x) { return (float)(__bf16)x; }
__device__ __forceinline__ float bflo(unsigned v) { return __builtin_bit_cast(float, v << 16); }
__device__ __forceinline__ float bfhi(unsigned v) { return __builtin_bit_cast(float, v & 0xffff0000u); }
__device__ __forceinline__ float bf1(unsigned v) { return __builtin_bit_cast(float, v << 16); }
__device__ __forceinline__ unsigned pack2(float lo, float hi) { bf16x2 v = {(__bf16)lo, (__bf16)hi}; return __builtin_bit_cast(unsigned, v); }
__device__ __forceinline__ float dot2(unsigned k, unsigned q, float acc) {
  return __builtin_amdgcn_fdot2_f32_bf16(__builtin_bit_cast(bf16x2, k), __builtin_bit_cast(bf16x2, q), acc, false);
}
__device__ __forceinline__ f32x4 mfma4(unsigned a0, unsigned a1, unsigned b0, unsigned b1, f32x4 c) {
  return __builtin_amdgcn_mfma_f32_4x4x4bf16_1k(__builtin_bit_cast(s16x4, u32x2{a0, a1}), __builtin_bit_cast(s16x4, u32x2{b0, b1}), c, 0, 0, 0);
}
__device__ __forceinline__ f32x4 mfma16(const u32x4& a, const u32x4& b, f32x4 c) {
  return __builtin_amdgcn_mfma_f32_16x16x32_bf16(__builtin_bit_cast(bf16x8, a), __builtin_bit_cast(bf16x8, b), c, 0, 0, 0);
}
typedef __attribute__((address_space(3))) s16x4* lds_s16x4_p;
__device__ __forceinline__ u32x2 ds_tr(unsigned a) { return __builtin_bit_cast(u32x2, __builtin_amdgcn_ds_read_tr16_b64_v4i16((lds_s16x4_p)(uintptr_t)a)); }
__device__ __forceinline__ float grp8_max(float x) {
  asm("s_nop 1\n\tv_max_f32_dpp %0, %0, %0 quad_perm:[1,0,3,2] row_mask:0xf bank_mask:0xf\n\t"
      "s_nop 1\n\tv_max_f32_dpp %0, %0, %0 quad_perm:[2,3,0,1] row_mask:0xf bank_mask:0xf\n\t"
      "s_nop 1\n\tv_max_f32_dpp %0, %0, %0 row_half_mirror row_mask:0xf bank_mask:0xf" : "+v"(x));
  return x;
}
__device__ __forceinline__ unsigned plo(unsigned r0, unsigned r1) { return __builtin_amdgcn_perm(r1, r0, 0x05040100u); }
__device__ __forceinline__ unsigned phi(unsigned r0, unsigned r1) { return __builtin_amdgcn_perm(r1, r0, 0x07060302u); }
template <int CTRL> __device__ __forceinline__ float dpp(float x) {
  return __builtin_bit_cast(float, __builtin_amdgcn_mov_dpp(__builtin_bit_cast(int, x), CTRL, 0xf, 0xf, true));
}
constexpr int XOR1 = 0xB1, XOR2 = 0x4E, HMIR = 0x141, MIR = 0x140, ROR8 = 0x128, ROR4 = 0x124;
__device__ __forceinline__ float sw32(float a, float b) {
  auto s = __builtin_amdgcn_permlane32_swap(__float_as_uint(a), __float_as_uint(b), false, false);
  return __uint_as_float(s[0]) + __uint_as_float(s[1]);
}
__device__ __forceinline__ float sw16(float a, float b) {
  auto s = __builtin_amdgcn_permlane16_swap(__float_as_uint(a), __float_as_uint(b), false, false);
  return __uint_as_float(s[0]) + __uint_as_float(s[1]);
}
__device__ __forceinline__ float xrow_max(float x) {
  auto s = __builtin_amdgcn_permlane16_swap(__float_as_uint(x), __float_as_uint(x), false, false);
  x = fmaxf(__uint_as_float(s[0]), __uint_as_float(s[1]));
  auto t = __builtin_amdgcn_permlane32_swap(__float_as_uint(x), __float_as_uint(x), false, false);
  return fmaxf(__uint_as_float(t[0]), __uint_as_float(t[1]));
}
__device__ __forceinline__ float grp8_sum(float x) { x += dpp<XOR1>(x); x += dpp<XOR2>(x); x += dpp<HMIR>(x); return x; }
__device__ __forceinline__ void lds_barrier() { asm volatile("s_waitcnt lgkmcnt(0)" ::: "memory"); __builtin_amdgcn_s_barrier(); asm volatile("" ::: "memory"); }
__device__ __forceinline__ float rdlane(float x, int l) { return __builtin_bit_cast(float, __builtin_amdgcn_readlane(__builtin_bit_cast(int, x), l)); }

__device__ __forceinline__ unsigned ln_pair(unsigned v, float mb, float rd) {
  const unsigned d = pack2(bflo(v) - mb, bfhi(v) - mb);
  return pack2(bflo(d) * rd, bfhi(d) * rd);
}
__device__ __forceinline__ unsigned ln_one(unsigned v, float mb, float rd) { return pack2(rbf(bf1(v) - mb) * rd, 0.f); }

__device__ __forceinline__ void dma_half(unsigned lds, const char* gb, unsigned voff) {
  unsigned keep;
  asm volatile(
      "s_mov_b32 %0, m0\n\t"
      "s_mov_b32 m0, %3\n\t"
      "s_nop 0\n\t"
      "global_load_lds_dwordx4 %1, %2 nt\n\t"
      "s_add_u32 m0, m0, 0x380\n\t"
      "s_nop 0\n\t"
      "global_load_lds_dwordx4 %1, %2 offset:128 nt\n\t"
      "s_add_u32 m0, m0, 0x380\n\t"
      "s_nop 0\n\t"
      "global_load_lds_dwordx4 %1, %2 offset:256 nt\n\t"
      "s_mov_b32 m0, %0"
      : "=&s"(keep) : "v"(voff), "s"(gb), "s"(lds) : "memory", "scc");
}
__device__ __forceinline__ void dma_step(unsigned lds, const char* kb, const char* vb, unsigned voff) {
  unsigned keep;
  asm volatile(
      "s_waitcnt lgkmcnt(0)\n\t"
      "s_mov_b32 %0, m0\n\t"
      "s_mov_b32 m0, %4\n\t"
      "s_nop 0\n\t"
      "global_load_lds_dwordx4 %1, %2 nt\n\t"
      "s_add_u32 m0, m0, 0x380\n\t"
      "s_nop 0\n\t"
      "global_load_lds_dwordx4 %1, %2 offset:128 nt\n\t"
      "s_add_u32 m0, m0, 0x380\n\t"
      "s_nop 0\n\t"
      "global_load_lds_dwordx4 %1, %2 offset:256 nt\n\t"
      "s_add_u32 m0, m0, 0x500\n\t"
      "s_nop 0\n\t"
      "global_load_lds_dwordx4 %1, %3 nt\n\t"
      "s_add_u32 m0, m0, 0x380\n\t"
      "s_nop 0\n\t"
      "global_load_lds_dwordx4 %1, %3 offset:128 nt\n\t"
      "s_add_u32 m0, m0, 0x380\n\t"
      "s_nop 0\n\t"
      "global_load_lds_dwordx4 %1, %3 offset:256 nt\n\t"
      "s_mov_b32 m0, %0"
      : "=&s"(keep) : "v"(voff), "s"(kb), "s"(vb), "s"(lds) : "memory", "scc");
}
#define LD4NT(dst, voff, base, off) asm volatile("global_load_dwordx4 %0, %1, %2 offset:%c3 nt" : "=v"(dst) : "v"(voff), "s"(base), "i"(off) : "memory")
#define LD4NTP(dst, voff, base, off) asm volatile("global_load_dwordx4 %0, %1, %2 offset:%c3 nt" : "+v"(dst) : "v"(voff), "s"(base), "i"(off) : "memory")
#define LD1NT(dst, voff, base, off) asm volatile("global_load_dword %0, %1, %2 offset:%c3 nt" : "=v"(dst) : "v"(voff), "s"(base), "i"(off) : "memory")
#define LD4(dst, voff, base, off) asm volatile("global_load_dwordx4 %0, %1, %2 offset:%c3" : "=v"(dst) : "v"(voff), "s"(base), "i"(off) : "memory")
#define LD2(dst, voff, base, off) asm volatile("global_load_dwordx2 %0, %1, %2 offset:%c3" : "=v"(dst) : "v"(voff), "s"(base), "i"(off) : "memory")
#define LD1(dst, voff, base, off) asm volatile("global_load_dword %0, %1, %2 offset:%c3" : "=v"(dst) : "v"(voff), "s"(base), "i"(off) : "memory")
#define LDU16(dst, voff, base, off) asm volatile("global_load_ushort %0, %1, %2 offset:%c3" : "=v"(dst) : "v"(voff), "s"(base), "i"(off) : "memory")
#define DSR32(dst, a, off) asm volatile("ds_read_b32 %0, %1 offset:%c2" : "=v"(dst) : "v"(a), "i"(off) : "memory")
#define DSR64(dst, a, off) asm volatile("ds_read_b64 %0, %1 offset:%c2" : "=v"(dst) : "v"(a), "i"(off) : "memory")
#define DSR128(dst, a, off) asm volatile("ds_read_b128 %0, %1 offset:%c2" : "=v"(dst) : "v"(a), "i"(off) : "memory")
#define DSR2X64(dst, a, o0, o1) asm volatile("ds_read2_b64 %0, %1 offset0:%c2 offset1:%c3" : "=v"(dst) : "v"(a), "i"(o0), "i"(o1) : "memory")
#define DSW64(a, v, off) asm volatile("ds_write_b64 %0, %1 offset:%c2" :: "v"(a), "v"(v), "i"(off) : "memory")
#define PIN(x) asm volatile("" : "+v"(x))
#define WAITVM(n) asm volatile("s_waitcnt vmcnt(" #n ")" ::: "memory")
#define WAITVMX(n) WAITVM(n)

__device__ __forceinline__ void put_unit(__amdgpu_buffer_rsrc_t rs, unsigned off, unsigned v0, unsigned v1, unsigned tA, unsigned tB) {
  u32x4 u = {tA ^ v1, v0, tB ^ v0, v1};
  __builtin_amdgcn_raw_buffer_store_b128(u, rs, off, 0, 16);
}
__device__ __forceinline__ bool unit_ok(const u32x4& v, unsigned tA, unsigned tB) { return ((v[0] ^ v[3]) == tA) && ((v[2] ^ v[1]) == tB); }
template <int N>
__device__ __forceinline__ void get_units(__amdgpu_buffer_rsrc_t rs, const unsigned (&off)[N], u32x4 (&v)[N], bool active, unsigned tA, unsigned tB) {
  bool ok = !active;
#pragma unroll
  for (int k = 0; k < N; ++k) asm volatile("" : "=v"(v[k]));
#pragma nounroll
  for (unsigned spin = 0; spin < SPIN_CAP; ++spin) {
    if (!ok) {
      asm volatile("" ::: "memory");
#pragma unroll
      for (int k = 0; k < N; ++k) v[k] = __builtin_amdgcn_raw_buffer_load_b128(rs, off[k], 0, 16);
      bool g = true;
#pragma unroll
      for (int k = 0; k < N; ++k) g = g && unit_ok(v[k], tA, tB);
      ok = g;
    }
    if (__all(ok)) break;
    __builtin_amdgcn_s_sleep(1);
  }
}
template <int N>
__device__ __forceinline__ void get_units_sel(__amdgpu_buffer_rsrc_t rs, const unsigned (&off)[N], u32x4 (&v)[N], unsigned tA, unsigned tB) {
  bool ok[N];
#pragma unroll
  for (int k = 0; k < N; ++k) ok[k] = false;
#pragma nounroll
  for (unsigned spin = 0; spin < SPIN_CAP; ++spin) {
    asm volatile("" ::: "memory");
#pragma unroll
    for (int k = 0; k < N; ++k) if (!ok[k]) v[k] = __builtin_amdgcn_raw_buffer_load_b128(rs, off[k], 0, 16);
    bool g = true;
#pragma unroll
    for (int k = 0; k < N; ++k) { ok[k] = unit_ok(v[k], tA, tB); g = g && ok[k]; }
    if (__all(g)) break;
    __builtin_amdgcn_s_sleep(1);
  }
}
__device__ __forceinline__ f32x4 red16_g4(const float (&a)[16]) {
  float t[8], u[4];
#pragma unroll
  for (int k = 0; k < 8; ++k) t[k] = sw32(a[k], a[8 + k]);
#pragma unroll
  for (int k = 0; k < 4; ++k) { u[k] = sw16(t[k], t[k + 4]); u[k] += dpp<ROR8>(u[k]); u[k] += dpp<ROR4>(u[k]); }
  return f32x4{u[0], u[1], u[2], u[3]};
}

__global__ __launch_bounds__(NTHR) void fused_kernel(const u16* __restrict__ x, const u16* __restrict__ Kc, const u16* __restrict__ Vc, const u16* __restrict__ wq,
                                                      const u16* __restrict__ wo, const u16* __restrict__ w1, const u16* __restrict__ b1, const u16* __restrict__ w2,
                                                      const u16* __restrict__ b2, u16* __restrict__ out, unsigned char* __restrict__ ws) {
  __shared__ __attribute__((aligned(16))) unsigned char lds[L_END];
  const int bid = blockIdx.x, loc = bid >> 3;
  const int h = (bid & 7) * 4 + (loc >> 3), sx = loc & 7, b = sx >> 2, j = sx & 3, g = b * NH + h;
  const int tid = threadIdx.x, lane = tid & 63, wave = __builtin_amdgcn_readfirstlane(tid >> 6), r = lane >> 3, c = lane & 7;
  const unsigned lds0 = __builtin_amdgcn_readfirstlane((unsigned)(uintptr_t)(__attribute__((address_space(3))) unsigned char*)lds);
  const __amdgpu_buffer_rsrc_t rs = __builtin_amdgcn_make_buffer_rsrc(ws, 0, W_END, 0x00020000);
  float* qp = reinterpret_cast<float*>(lds + L_QP);
  unsigned* ql = reinterpret_cast<unsigned*>(lds + L_Q);
  unsigned* misc = reinterpret_cast<unsigned*>(lds + L_MISC);
  float* t1 = reinterpret_cast<float*>(lds + L_T1);
  unsigned* t2 = reinterpret_cast<unsigned*>(lds + L_T2);
#define STAMP(i)
  STAMP(0);
  const bool is_mlp = j == 0, is_op = j != 0 && sx != 7, is_red = is_op && wave == RW;
  const int jb = g, oi = sx - 1 - b, rid = h * 5 + oi;
  constexpr int nstep = 256;
  const char* kblk = reinterpret_cast<const char*>(Kc) + ((size_t)g * SK + (size_t)j * 8) * 384;
  const char* vblk = reinterpret_cast<const char*>(Vc) + ((size_t)g * SK + (size_t)j * 8) * 384;
  const int rot = g * ROTG;
  auto so = [&](int n) { return (size_t)((n + rot) & 255) * 12288; };
  const unsigned ring = lds0 + L_RING + wave * RINGW;
  const unsigned kvoff = r * 384 + c * 16;
  const int g4 = lane >> 2, c4 = lane & 3, rg = lane >> 4, c16 = lane & 15;
  unsigned bsm = 0, rx = 0, rb2 = 0;
  const char* w1c = reinterpret_cast<const char*>(w1) + jb * 48;
  const char* w2c = reinterpret_cast<const char*>(w2) + (size_t)jb * 30720;
  unsigned tA = 0, tB = 1;
  unsigned recw = L_T1 + wave * 1024 + (4 + c * 24 + 12 * (lane >> 5) + 6 * ((lane >> 4) & 1)) * 4, recm = lds0 + L_T1 + c * 1024;
  unsigned recr = lds0 + L_T1 + (tid < 96 ? 4 + (((2 * tid) & 63) >> 3) * 24 + ((2 * tid) >> 6) * 8 + ((2 * tid) & 7) : 0) * 4, p1o = W_P1 + (unsigned)(h * 8 + sx) * 1792 + tid * 16;
  float m = -INFINITY, l = 0.f;
  float acc[24];
  u32x4 wov[6];
#pragma unroll
  for (int k = 0; k < 24; ++k) acc[k] = 0.f;
  {
  u32x2 ep;
  if (wave == NW - 1) {
    asm volatile("global_load_dwordx2 %0, %1, %2 sc1" : "=v"(ep) : "v"(0u), "s"(ws) : "memory");
    if (lane == 0) { misc[5] = 0; misc[6] = 0; }
  }
  const int qi = (0x2F41F530 >> (4 * sx)) & 15;
  if (qi < 6) {
    u32x4 wv[5];
    const int k0 = (qi >= 3 ? 320 : 0) + 40 * wave;
    if (wave == 0) {
      unsigned keep;
      asm volatile(
          "s_mov_b32 %0, m0\n\t"
          "s_mov_b32 m0, %3\n\t"
          "s_nop 0\n\t"
          "global_load_lds_dwordx4 %1, %2\n\t"
          "global_load_lds_dwordx4 %1, %2 offset:1024\n\t"
          "s_mov_b32 exec_hi, 0\n\t"
          "global_load_lds_dwordx4 %1, %2 offset:2048\n\t"
          "s_mov_b32 exec_hi, -1\n\t"
          "s_mov_b32 m0, %0"
          : "=&s"(keep) : "v"(lane * 16), "s"(x), "s"(lds0 + L_T2) : "memory");
    }
    const unsigned wcol = (unsigned)(h * 384 + (qi >= 3 ? qi - 3 : qi) * 128 + c * 16);
    const unsigned wa = (unsigned)(k0 + 4 * r) * 12288u + wcol, wb = (unsigned)(k0 + 32 + r) * 12288u + wcol;
    const char* wqc = reinterpret_cast<const char*>(wq);
    LD4NT(wv[0], wa, wqc, 0); LD4NT(wv[1], wa, wqc + 12288, 0); LD4NT(wv[2], wa, wqc + 24576, 0); LD4NT(wv[3], wa, wqc + 36864, 0); LD4NT(wv[4], wb, wqc, 0);
    if (wave == 0) WAITVM(5);
    lds_barrier();
    dma_half(ring, kblk + so(wave), kvoff);
    dma_half(ring + 3072, vblk + so(wave), kvoff);
    unsigned xp[2][3];
    {
      const unsigned char* xl = lds + L_T2;
      u32x2 xr[5];
#pragma unroll
      for (int i = 0; i < 5; ++i) xr[i] = *reinterpret_cast<const u32x2*>(xl + (lane >> 5) * 1280 + (lane & 31) * 8 + i * 256);
      u32x2 xo[2];
      unsigned xs[2];
#pragma unroll
      for (int bb = 0; bb < 2; ++bb) { xo[bb] = *reinterpret_cast<const u32x2*>(xl + bb * 1280 + (k0 + 4 * r) * 2); xs[bb] = *reinterpret_cast<const u16*>(xl + bb * 1280 + (k0 + 32 + r) * 2); }
      float s0 = 0.f, s1 = 0.f;
#pragma unroll
      for (int i = 0; i < 5; ++i) { s0 = dot2(xr[i][0], 0x3f803f80u, s0); s1 = dot2(xr[i][1], 0x3f803f80u, s1); }
      float sm = grp8_sum(s0 + s1); sm += dpp<MIR>(sm); sm = sw16(sm, sm);
      const float mean = sm * (1.0f / DM);
      f32x2 q0 = {0.f, 0.f}, q1 = {0.f, 0.f};
#pragma unroll
      for (int i = 0; i < 5; ++i) {
        const f32x2 e0 = f32x2{bflo(xr[i][0]), bfhi(xr[i][0])} - mean, e1 = f32x2{bflo(xr[i][1]), bfhi(xr[i][1])} - mean;
        q0 = __builtin_elementwise_fma(e0, e0, q0); q1 = __builtin_elementwise_fma(e1, e1, q1);
      }
      q0 += q1;
      float qv = grp8_sum(q0[0] + q0[1]); qv += dpp<MIR>(qv); qv = sw16(qv, qv);
      const float mbv = rbf(mean), rdv = __builtin_amdgcn_rcpf(rbf(__builtin_amdgcn_sqrtf(rbf(rbf(qv * (1.0f / DM)) + 1e-5f))));
#pragma unroll
      for (int bb = 0; bb < 2; ++bb) {
        const float mb = rdlane(mbv, 32 * bb), rd = rdlane(rdv, 32 * bb);
        xp[bb][0] = ln_pair(xo[bb][0], mb, rd); xp[bb][1] = ln_pair(xo[bb][1], mb, rd); xp[bb][2] = ln_one(xs[bb], mb, rd);
      }
    }
    asm volatile("" : "+v"(xp[0][0]), "+v"(xp[0][1]), "+v"(xp[0][2]), "+v"(xp[1][0]), "+v"(xp[1][1]), "+v"(xp[1][2]));
    asm volatile("s_waitcnt vmcnt(6) ; WQ_LANDED %0 %1 %2 %3 %4" : "+v"(wv[0]), "+v"(wv[1]), "+v"(wv[2]), "+v"(wv[3]), "+v"(wv[4]), "+v"(xp[0][0]), "+v"(xp[0][1]), "+v"(xp[0][2]), "+v"(xp[1][0]), "+v"(xp[1][1]), "+v"(xp[1][2])::"memory");
    STAMP(1);
    float a[16];
#pragma unroll
    for (int k = 0; k < 4; ++k) {
      const unsigned p0 = plo(wv[0][k], wv[1][k]), p1 = phi(wv[0][k], wv[1][k]), p2 = plo(wv[2][k], wv[3][k]), p3 = phi(wv[2][k], wv[3][k]);
      const unsigned p4 = wv[4][k] & 0xffffu, p5 = wv[4][k] >> 16;
#pragma unroll
      for (int bb = 0; bb < 2; ++bb) {
        a[bb * 8 + 2 * k] = dot2(p4, xp[bb][2], dot2(p2, xp[bb][1], dot2(p0, xp[bb][0], 0.f)));
        a[bb * 8 + 2 * k + 1] = dot2(p5, xp[bb][2], dot2(p3, xp[bb][1], dot2(p1, xp[bb][0], 0.f)));
      }
    }
    float t[8], u[4];
#pragma unroll
    for (int k = 0; k < 8; ++k) t[k] = sw32(a[k], a[8 + k]);
#pragma unroll
    for (int k = 0; k < 4; ++k) { u[k] = sw16(t[k], t[k + 4]); u[k] += dpp<ROR8>(u[k]); }
    if ((lane & 8) == 0) *reinterpret_cast<f32x4*>(t1 + wave * 128 + (lane >> 5) * 64 + c * 8 + ((lane >> 4) & 1) * 4) = f32x4{u[0], u[1], u[2], u[3]};
  } else {
    dma_half(ring, kblk + so(wave), kvoff);
    dma_half(ring + 3072, vblk + so(wave), kvoff);
    if (wave == NW - 1) WAITVM(6);
  }
  lds_barrier();
  STAMP(2);
  if (wave == NW - 1) {
    asm volatile("; EP_LANDED %0" : "+v"(ep));
    const u64 e1 = (((u64)ep[1] << 32) | ep[0]) + 1ull;
    const unsigned tA = (unsigned)e1, tB = (((unsigned)e1 * 0x9E3779B1u) ^ (unsigned)(e1 >> 32)) | 1u;
    if (qi < 6) {
      f32x2 a = {0.f, 0.f};
#pragma unroll
      for (int w = 0; w < NW; ++w) a += *reinterpret_cast<const f32x2*>(t1 + w * 128 + lane * 2);
      put_unit(rs, W_QX + (unsigned)((h * 6 + qi) * 64 + lane) * 16, __float_as_uint(a[0]), __float_as_uint(a[1]), tA, tB);
    }
    if (lane == 0) { misc[0] = (3 + 3 + 0) * NW; misc[1] = tA; misc[2] = tB; misc[3] = (unsigned)e1; misc[4] = (unsigned)(e1 >> 32); }
    unsigned off[3];
#pragma unroll
    for (int k = 0; k < 3; ++k) off[k] = W_QX + (unsigned)((h * 6 + (lane >> 5) * 3 + k) * 64 + b * 32 + (lane & 31)) * 16;
    u32x4 v[3];
    get_units_sel<3>(rs, off, v, tA, tB);
#pragma unroll
    for (int k = 0; k < 3; ++k) reinterpret_cast<u16*>(ql)[(k * 32 + (lane & 31)) * 2 + (lane >> 5)] = (u16)(pack2(sw32(__uint_as_float(v[k][1]), __uint_as_float(v[k][3])), 0.f) & 0xffffu);
    asm volatile("s_waitcnt lgkmcnt(0)" ::: "memory");
    if (lane == 0) __hip_atomic_store(misc + 5, 1u, __ATOMIC_RELAXED, __HIP_MEMORY_SCOPE_WORKGROUP);
    STAMP(9);
  }
  {
    if (is_mlp) {
      const unsigned bo = (unsigned)(jb * 24 + (lane < 48 ? lane % 24 : 0)) * 2;
      LDU16(bsm, bo, b1, 0);
      LD1(rx, (unsigned)(lane & 1) * (639u * 3072u), w1c, 0);
      LD1(rb2, (unsigned)(lane & 1) * 30716u, w2c, 0);
    } else if (is_red) {
      const unsigned col = (unsigned)((rid % 80) * 8 + 2 * c4 + (lane >> 5));
      LDU16(rx, col * 2, x + (rid / 80) * DM, 0);
      LDU16(rb2, col * 2, b2, 0);
    }
    WAITVMX(PD); dma_half(ring + 6144, kblk + so(wave + 8), kvoff);
    WAITVMX(PD); dma_half(ring + 9216, vblk + so(wave + 8), kvoff);
    WAITVMX(PD); dma_half(ring + 12288, kblk + so(wave + 16), kvoff);
    WAITVMX(PD); dma_half(ring + 15360, vblk + so(wave + 16), kvoff);
    if (is_op) {
      const char* woc = reinterpret_cast<const char*>(wo) + (size_t)(h * DH + 24 * wave) * 1280 + oi * 256;
      const unsigned oa = (unsigned)(4 * rg) * 1280u + c16 * 16, ob = (unsigned)(16 + 2 * rg) * 1280u + c16 * 16;
      WAITVMX(PD); LD4NT(wov[0], oa, woc, 0); LD4NT(wov[1], oa, woc, 1280); LD4NT(wov[2], oa, woc, 2560);
      WAITVMX(PD); LD4NT(wov[3], oa, woc, 3840); LD4NT(wov[4], ob, woc, 0); LD4NT(wov[5], ob, woc, 1280);
    }
  }
  while (__hip_atomic_load(misc + 5, __ATOMIC_RELAXED, __HIP_MEMORY_SCOPE_WORKGROUP) == 0) __builtin_amdgcn_s_sleep(1);
  asm volatile("" ::: "memory");
  STAMP(3);
  tA = misc[1]; tB = misc[2];
  {
    u32x4 q[3];
#pragma unroll
    for (int t = 0; t < 3; ++t) q[t] = *reinterpret_cast<const u32x4*>(ql + t * 32 + c * 4);
    constexpr float SC = 0.07216878364870322f, L2E = 1.4426950408889634f;
    int nxt = (3 + 0) * NW + wave;
    int slot = 0;
    const unsigned char* rp = lds + L_RING + wave * RINGW + lane * 16;
    auto step = [&](const u32x4 (&kb)[3], const u32x4 (&vb)[3]) {
      float d = 0.f;
#pragma unroll
      for (int t = 0; t < 3; ++t)
#pragma unroll
        for (int k = 0; k < 4; ++k) d = dot2(kb[t][k], q[t][k], d);
      d = grp8_sum(d);
      const float s_ = rbf(rbf(d) * SC) * L2E;
      if (__any(s_ > m)) {
        const float mx = fmaxf(m, xrow_max(fmaxf(s_, dpp<ROR8>(s_)))), al = __builtin_amdgcn_exp2f(m - mx);
        l *= al;
#pragma unroll
        for (int k = 0; k < 24; ++k) acc[k] *= al;
        m = mx;
      }
      const float p = __builtin_amdgcn_exp2f(s_ - m);
      l += p;
#pragma unroll
      for (int t = 0; t < 3; ++t)
#pragma unroll
        for (int k = 0; k < 4; ++k) {
          acc[t * 8 + 2 * k] = fmaf(p, bflo(vb[t][k]), acc[t * 8 + 2 * k]);
          acc[t * 8 + 2 * k + 1] = fmaf(p, bfhi(vb[t][k]), acc[t * 8 + 2 * k + 1]);
        }
    };
    auto iter = [&](bool dyn) {
      asm volatile("s_waitcnt vmcnt(12)" ::: "memory");
      u32x4 kb[3], vb[3];
#pragma unroll
      for (int t = 0; t < 3; ++t) { kb[t] = *reinterpret_cast<const u32x4*>(rp + slot * 6144 + t * 1024); vb[t] = *reinterpret_cast<const u32x4*>(rp + slot * 6144 + 3072 + t * 1024); }
      dma_step(ring + slot * 6144, kblk + so(nxt), vblk + so(nxt), kvoff);
      if (!dyn) nxt += NW;
      else {
        int n2 = 0;
        if (lane == 0) n2 = __hip_atomic_fetch_add(misc, 1u, __ATOMIC_RELAXED, __HIP_MEMORY_SCOPE_WORKGROUP);
        nxt = __builtin_amdgcn_readfirstlane(n2);
      }
      step(kb, vb);
      slot = slot == 3 - 1 ? 0 : slot + 1;
    };
    iter(false); iter(false); iter(true);
    {
      asm volatile("s_waitcnt vmcnt(18) ; X_LANDED %0 %1 %2 %3 %4 %5 %6 %7 %8" : "+v"(bsm), "+v"(rx), "+v"(rb2), "+v"(wov[0]), "+v"(wov[1]), "+v"(wov[2]), "+v"(wov[3]), "+v"(wov[4]), "+v"(wov[5])::"memory");
    }
#pragma nounroll
    while (nxt < nstep) iter(true);
    PIN(recw); PIN(recm); PIN(recr); PIN(p1o);
#pragma nounroll
    for (int wc = 2; wc >= 0; --wc) {
      if (wc == 2) asm volatile("s_waitcnt vmcnt(12)" ::: "memory");
      else if (wc == 1) asm volatile("s_waitcnt vmcnt(6)" ::: "memory");
      else asm volatile("s_waitcnt vmcnt(0)" ::: "memory");
      u32x4 kb[3], vb[3];
#pragma unroll
      for (int t = 0; t < 3; ++t) { kb[t] = *reinterpret_cast<const u32x4*>(rp + slot * 6144 + t * 1024); vb[t] = *reinterpret_cast<const u32x4*>(rp + slot * 6144 + 3072 + t * 1024); }
      step(kb, vb);
      slot = slot == 3 - 1 ? 0 : slot + 1;
    }
    STAMP(4);
  }
  }
  {
    float t12[12], t6[6];
    l += dpp<ROR8>(l); l = sw16(l, l); l = sw32(l, l);
#pragma unroll
    for (int k = 0; k < 12; ++k) t12[k] = sw32(acc[k], acc[k + 12]);
#pragma unroll
    for (int k = 0; k < 6; ++k) { t6[k] = sw16(t12[k], t12[k + 6]); t6[k] += dpp<ROR8>(t6[k]); }
    if ((lane & 8) == 0) {
      float* p = reinterpret_cast<float*>(lds + recw);
#pragma unroll
      for (int k = 0; k < 3; ++k) *reinterpret_cast<f32x2*>(p + 2 * k) = f32x2{t6[2 * k], t6[2 * k + 1]};
      if (lane == 0) *reinterpret_cast<f32x2*>(p - 4) = f32x2{m, l};
    }
  }
  lds_barrier();
  if (tid < 128) {
    float mw;
    f32x2 a[NW];
    DSR32(mw, recm, 0);
#pragma unroll
    for (int w = 0; w < NW; ++w) DSR64(a[w], recr, w * 1024);
    asm volatile("s_waitcnt lgkmcnt(8)" : "+v"(mw)::"memory");
    const float M = grp8_max(mw);
    float wt = __builtin_amdgcn_exp2f(mw - M);
    asm volatile("s_waitcnt lgkmcnt(0)" : "+v"(wt), "+v"(a[0]), "+v"(a[1]), "+v"(a[2]), "+v"(a[3]), "+v"(a[4]), "+v"(a[5]), "+v"(a[6]), "+v"(a[7])::"memory");
    f32x2 o0 = {0.f, 0.f}, o1 = {0.f, 0.f};
#pragma unroll
    for (int w = 0; w < NW; w += 2) {
      const float w0 = rdlane(wt, w), w1_ = rdlane(wt, w + 1);
      o0 = __builtin_elementwise_fma(f32x2{w0, w0}, a[w], o0); o1 = __builtin_elementwise_fma(f32x2{w1_, w1_}, a[w + 1], o1);
    }
    o0 += o1;
    if (tid < 97) put_unit(rs, p1o, __float_as_uint(tid < 96 ? o0[0] : M), __float_as_uint(o0[1]), tA, tB);
  }
  if (tid == 128) put_unit(rs, W_DN + (unsigned)bid * 16, 0u, 0u, tA, tB);
  if (is_op) {
  u32x4 wa[6];
  {
#pragma unroll
    for (int k = 0; k < 6; ++k) {
      const int row = 24 * wave + (k < 4 ? 4 * rg + k : 16 + 2 * rg + (k - 4));
      *reinterpret_cast<u32x4*>(lds + L_RING + row * 256 + (((c16 >> 1) ^ (row & 7)) << 5) + (c16 & 1) * 16) = wov[k];
    }
    lds_barrier();
    const int rr = lane >> 2;
    const unsigned ta = lds0 + L_RING + rr * 256 + ((wave ^ (rr & 7)) << 5) + c4 * 8;
#pragma unroll
    for (int t = 0; t < 6; ++t) {
      const u32x2 lo = ds_tr(ta + t * 8192), hi = ds_tr(ta + t * 8192 + 4096);
      wa[t] = u32x4{lo[0], lo[1], hi[0], hi[1]};
    }
    asm volatile("" : "+v"(wa[0]), "+v"(wa[1]), "+v"(wa[2]), "+v"(wa[3]), "+v"(wa[4]), "+v"(wa[5]));
  }
  unsigned ora = lds0 + L_T2 + (lane & 1) * 384 + rg * 8, y2o = W_Y2 + (unsigned)(h * 5 + oi) * 2048 + (unsigned)((lane & 1) * 64 + 8 * wave + 2 * rg + ((lane >> 1) & 1)) * 16;
  PIN(ora); PIN(y2o);
  if (wave >= 5) {
    const int t3 = tid - 320, bb = t3 >= 96, u = t3 - 96 * bb;
    const unsigned base = W_P1 + (unsigned)(h * 8 + bb * 4) * 1792;
    u32x4 v[4], f[4];
#pragma unroll
    for (int jj = 0; jj < 4; ++jj) { f[jj] = __builtin_amdgcn_raw_buffer_load_b128(rs, base + jj * 1792 + 96 * 16, 0, 16); v[jj] = __builtin_amdgcn_raw_buffer_load_b128(rs, base + jj * 1792 + u * 16, 0, 16); }
#pragma nounroll
    for (unsigned spin = 0; spin < SPIN_CAP; ++spin) {
      bool okf[4], okv[4];
      int miss = 0;
      bool all = true;
#pragma unroll
      for (int jj = 0; jj < 4; ++jj) { okf[jj] = unit_ok(f[jj], tA, tB); okv[jj] = unit_ok(v[jj], tA, tB); miss += okf[jj] ? 0 : 1; all = all && okf[jj] && okv[jj]; }
      if (__all(all)) break;
      __builtin_amdgcn_s_sleep(1);
      asm volatile("" ::: "memory");
#pragma unroll
      for (int jj = 0; jj < 4; ++jj) {
        if (!okf[jj]) f[jj] = __builtin_amdgcn_raw_buffer_load_b128(rs, base + jj * 1792 + 96 * 16, 0, 16);
        if (!okv[jj] && (okf[jj] || miss <= 1)) v[jj] = __builtin_amdgcn_raw_buffer_load_b128(rs, base + jj * 1792 + u * 16, 0, 16);
      }
    }
    const float M = fmaxf(fmaxf(__uint_as_float(f[0][1]), __uint_as_float(f[1][1])), fmaxf(__uint_as_float(f[2][1]), __uint_as_float(f[3][1])));
    float L = 0.f, o0 = 0.f, o1 = 0.f;
#pragma unroll
    for (int jj = 0; jj < 4; ++jj) {
      const float wt = __builtin_amdgcn_exp2f(__uint_as_float(f[jj][1]) - M);
      L = fmaf(wt, __uint_as_float(f[jj][3]), L); o0 = fmaf(wt, __uint_as_float(v[jj][1]), o0); o1 = fmaf(wt, __uint_as_float(v[jj][3]), o1);
    }
    const float rl = __builtin_amdgcn_rcpf(L);
    t2[t3] = pack2(o0 * rl, o1 * rl);
  }
  lds_barrier();
  STAMP(5);
  {
    u32x4 ob[6];
#pragma unroll
    for (int t = 0; t < 6; ++t) DSR2X64(ob[t], ora, 8 * t, 8 * t + 4);
    f32x4 d = {0.f, 0.f, 0.f, 0.f};
    asm volatile("s_waitcnt lgkmcnt(3)" : "+v"(ob[0]), "+v"(ob[1]), "+v"(ob[2])::"memory");
#pragma unroll
    for (int t = 0; t < 3; ++t) d = mfma16(wa[t], ob[t], d);
    asm volatile("s_waitcnt lgkmcnt(0)" : "+v"(ob[3]), "+v"(ob[4]), "+v"(ob[5])::"memory");
#pragma unroll
    for (int t = 3; t < 6; ++t) d = mfma16(wa[t], ob[t], d);
    if (c16 < 4) put_unit(rs, y2o, __float_as_uint((lane & 2) ? d[2] : d[0]), __float_as_uint((lane & 2) ? d[3] : d[1]), tA, tB);
  }
  STAMP(6);
    if (is_red) {
      const int bq = rid / 80, cg = rid % 80;
      float hv;
      {
        unsigned off[2];
#pragma unroll
        for (int k = 0; k < 2; ++k) off[k] = W_Y2 + (unsigned)((16 * k + g4) * 5 + (cg >> 4)) * 2048 + (unsigned)(bq * 64 + (cg & 15) * 4 + c4) * 16;
        unsigned h3o = W_H3 + (unsigned)(lane >> 1) * H3S + (unsigned)(rid * 2 + (lane & 1)) * 16;
        float rxf = bf1(rx);
        PIN(h3o); PIN(rxf);
        u32x4 v[2];
        get_units<2>(rs, off, v, true, tA, tB);
        const float a0 = __uint_as_float(v[0][1]) + __uint_as_float(v[1][1]);
        const float a1 = __uint_as_float(v[0][3]) + __uint_as_float(v[1][3]);
        float s = sw32(a0, a1); s = sw16(s, s); s += dpp<ROR8>(s); s += dpp<ROR4>(s);
        hv = rbf(rxf + rbf(s));
        auto e = __builtin_amdgcn_permlane32_swap(__float_as_uint(hv), __float_as_uint(hv), false, false);
        const unsigned pk = (e[0] >> 16) | (e[1] & 0xffff0000u);
        const unsigned u0 = (unsigned)__builtin_amdgcn_readlane((int)pk, 0), u1 = (unsigned)__builtin_amdgcn_readlane((int)pk, 1), u2 = (unsigned)__builtin_amdgcn_readlane((int)pk, 2), u3 = (unsigned)__builtin_amdgcn_readlane((int)pk, 3);
        if (lane < 2 * H3R) put_unit(rs, h3o, (lane & 1) ? u2 : u0, (lane & 1) ? u3 : u1, tA, tB);
      }
      STAMP(7);
      {
        unsigned off[4];
#pragma unroll
        for (int k = 0; k < 4; ++k) off[k] = W_O4 + (unsigned)(16 * k + g4) * 10240 + (unsigned)(bq * 320 + cg * 4 + c4) * 16;
        unsigned oo = (unsigned)(bq * 320 + cg * 4 + lane) * 4;
        float rbf2 = bf1(rb2);
        PIN(oo); PIN(rbf2);
        u32x4 v[4];
        get_units<4>(rs, off, v, true, tA, tB);
        const float a0 = (__uint_as_float(v[0][1]) + __uint_as_float(v[1][1])) + (__uint_as_float(v[2][1]) + __uint_as_float(v[3][1]));
        const float a1 = (__uint_as_float(v[0][3]) + __uint_as_float(v[1][3])) + (__uint_as_float(v[2][3]) + __uint_as_float(v[3][3]));
        float s = sw32(a0, a1); s = sw16(s, s); s += dpp<ROR8>(s); s += dpp<ROR4>(s);
        const float ov = rbf(hv + rbf(rbf(s) + rbf2));
        auto e = __builtin_amdgcn_permlane32_swap(__float_as_uint(ov), __float_as_uint(ov), false, false);
        if (lane < 4) *reinterpret_cast<unsigned*>(reinterpret_cast<char*>(out) + oo) = (e[0] >> 16) | (e[1] & 0xffff0000u);
        if (rid == 0 && lane == 0) __hip_atomic_store(reinterpret_cast<u64*>(ws + W_EPOCH), ((u64)misc[4] << 32) | misc[3], __ATOMIC_RELAXED, __HIP_MEMORY_SCOPE_AGENT);
      }
    }
  } else if (is_mlp && wave == 7) {
    const unsigned smo = W_DN + (unsigned)((bid & 192) + lane) * 16;
#pragma nounroll
    for (unsigned spin = 0; spin < SPIN_CAP; ++spin) {
      const u32x4 v = __builtin_amdgcn_raw_buffer_load_b128(rs, smo, 0, 16);
      if (__builtin_popcountll(__ballot(unit_ok(v, tA, tB))) >= THR) break;
      __builtin_amdgcn_s_sleep(4);
    }
    if (lane == 0) __hip_atomic_store(misc + 6, 1u, __ATOMIC_RELAXED, __HIP_MEMORY_SCOPE_WORKGROUP);
  } else if (is_mlp && wave == 5) {
    lds_barrier();
    unsigned off[5];
#pragma unroll
    for (int k = 0; k < 5; ++k) off[k] = W_H3 + (unsigned)(jb % H3R) * H3S + (unsigned)((lane >> 5) * 160 + 32 * k + (lane & 31)) * 16;
    u32x4 v[5];
    get_units<5>(rs, off, v, true, tA, tB);
    float s0 = 0.f, s1 = 0.f;
#pragma unroll
    for (int k = 0; k < 5; ++k) { s0 = dot2(v[k][1], 0x3f803f80u, s0); s1 = dot2(v[k][3], 0x3f803f80u, s1); }
    float s = grp8_sum(s0 + s1); s += dpp<MIR>(s); s = sw16(s, s);
    const float mean = s * (1.0f / DM);
    f32x2 q0 = {0.f, 0.f}, q1 = {0.f, 0.f};
#pragma unroll
    for (int k = 0; k < 5; ++k) {
      const f32x2 e0 = f32x2{bflo(v[k][1]), bfhi(v[k][1])} - mean, e1 = f32x2{bflo(v[k][3]), bfhi(v[k][3])} - mean;
      q0 = __builtin_elementwise_fma(e0, e0, q0); q1 = __builtin_elementwise_fma(e1, e1, q1);
    }
    q0 += q1;
    float q = grp8_sum(q0[0] + q0[1]); q += dpp<MIR>(q); q = sw16(q, q);
    const float rd = __builtin_amdgcn_rcpf(rbf(__builtin_amdgcn_sqrtf(rbf(rbf(q * (1.0f / DM)) + 1e-5f))));
    if ((lane & 31) == 0) *reinterpret_cast<f32x2*>(lds + L_ST + (lane >> 5) * 8) = f32x2{rbf(mean), rd};
    lds_barrier();
  } else if (is_mlp && wave < 5) {
    const int li = lane < 48 ? lane : 0, lb = li >= 24, hd = li - 24 * lb, pos = (hd & 24) + 4 * (hd & 1) + ((hd & 7) >> 1);
    float csum = 0.f;
    u32x4 w1p[8];
    unsigned w2v[24];
    {
      while (__hip_atomic_load(misc + 6, __ATOMIC_RELAXED, __HIP_MEMORY_SCOPE_WORKGROUP) == 0) __builtin_amdgcn_s_sleep(1);
      unsigned w1v[32];
      {
        const unsigned w1a = (unsigned)(128 * wave + 8 * rg) * 3072u + (c16 < 12 ? c16 : 11) * 4;
#pragma unroll
        for (int k = 0; k < 32; k += 2) { const char* p = w1c + ((k >> 3) * 32 + (k & 7)) * 3072; LD1NT(w1v[k], w1a, p, 0); LD1NT(w1v[k + 1], w1a, p, 3072); }
        const unsigned o2 = (unsigned)tid * 4;
#pragma unroll
        for (int k = 0; k < 24; k += 3) { LD1NT(w2v[k], o2, w2c + k * 1280, 0); LD1NT(w2v[k + 1], o2, w2c + k * 1280, 1280); LD1NT(w2v[k + 2], o2, w2c + k * 1280, 2560); }
      }
      asm volatile("s_waitcnt vmcnt(24) ; W1_LANDED %0 %1 %2 %3 %4 %5 %6 %7 %8 %9 %10 %11 %12 %13 %14 %15" : "+v"(w1v[0]), "+v"(w1v[1]), "+v"(w1v[2]), "+v"(w1v[3]), "+v"(w1v[4]), "+v"(w1v[5]), "+v"(w1v[6]), "+v"(w1v[7]),
                   "+v"(w1v[8]), "+v"(w1v[9]), "+v"(w1v[10]), "+v"(w1v[11]), "+v"(w1v[12]), "+v"(w1v[13]), "+v"(w1v[14]), "+v"(w1v[15])::"memory");
      asm volatile("; W1B_LANDED %0 %1 %2 %3 %4 %5 %6 %7 %8 %9 %10 %11 %12 %13 %14 %15" : "+v"(w1v[16]), "+v"(w1v[17]), "+v"(w1v[18]), "+v"(w1v[19]), "+v"(w1v[20]), "+v"(w1v[21]), "+v"(w1v[22]), "+v"(w1v[23]),
                   "+v"(w1v[24]), "+v"(w1v[25]), "+v"(w1v[26]), "+v"(w1v[27]), "+v"(w1v[28]), "+v"(w1v[29]), "+v"(w1v[30]), "+v"(w1v[31])::"memory");
      STAMP(5);
#pragma unroll
      for (int t = 0; t < 4; ++t)
#pragma unroll
        for (int i = 0; i < 4; ++i) { w1p[2 * t][i] = plo(w1v[8 * t + 2 * i], w1v[8 * t + 2 * i + 1]); w1p[2 * t + 1][i] = phi(w1v[8 * t + 2 * i], w1v[8 * t + 2 * i + 1]); }
      f32x4 c0 = {0.f, 0.f, 0.f, 0.f}, c1 = {0.f, 0.f, 0.f, 0.f};
      const u32x4 ones = {0x3f803f80u, 0x3f803f80u, 0x3f803f80u, 0x3f803f80u};
#pragma unroll
      for (int t = 0; t < 4; ++t) { c0 = mfma16(w1p[2 * t], ones, c0); c1 = mfma16(w1p[2 * t + 1], ones, c1); }
      if (c16 == 0 && rg < 3) { f32x4* p = reinterpret_cast<f32x4*>(lds + L_CS + (wave * 24 + rg * 8) * 4); p[0] = c0; p[1] = c1; }
      asm volatile("s_waitcnt vmcnt(0) ; W2_LANDED %0 %1 %2 %3 %4 %5 %6 %7 %8 %9 %10 %11 %12 %13 %14 %15 %16 %17 %18 %19 %20 %21 %22 %23" : "+v"(w2v[0]), "+v"(w2v[1]), "+v"(w2v[2]), "+v"(w2v[3]), "+v"(w2v[4]), "+v"(w2v[5]), "+v"(w2v[6]), "+v"(w2v[7]), "+v"(w2v[8]), "+v"(w2v[9]), "+v"(w2v[10]), "+v"(w2v[11]),
                   "+v"(w2v[12]), "+v"(w2v[13]), "+v"(w2v[14]), "+v"(w2v[15]), "+v"(w2v[16]), "+v"(w2v[17]), "+v"(w2v[18]), "+v"(w2v[19]), "+v"(w2v[20]), "+v"(w2v[21]), "+v"(w2v[22]), "+v"(w2v[23]));
      STAMP(6);
#pragma unroll
      for (int i = 0; i < 12; ++i) { const unsigned lo = plo(w2v[2 * i], w2v[2 * i + 1]), hi = phi(w2v[2 * i], w2v[2 * i + 1]); w2v[2 * i] = lo; w2v[2 * i + 1] = hi; }
      lds_barrier();
#pragma unroll
      for (int w = 0; w < 5; ++w) csum += reinterpret_cast<const float*>(lds + L_CS)[w * 24 + pos];
      asm volatile("" : "+v"(csum), "+v"(w2v[0]), "+v"(w2v[1]), "+v"(w2v[2]), "+v"(w2v[3]), "+v"(w2v[4]), "+v"(w2v[5]), "+v"(w2v[6]), "+v"(w2v[7]), "+v"(w2v[8]), "+v"(w2v[9]), "+v"(w2v[10]), "+v"(w2v[11]),
                   "+v"(w2v[12]), "+v"(w2v[13]), "+v"(w2v[14]), "+v"(w2v[15]), "+v"(w2v[16]), "+v"(w2v[17]), "+v"(w2v[18]), "+v"(w2v[19]), "+v"(w2v[20]), "+v"(w2v[21]), "+v"(w2v[22]), "+v"(w2v[23]));
      asm volatile("" : "+v"(w1p[0]), "+v"(w1p[1]), "+v"(w1p[2]), "+v"(w1p[3]), "+v"(w1p[4]), "+v"(w1p[5]), "+v"(w1p[6]), "+v"(w1p[7]));
    }
    unsigned hwa = lds0 + L_T2 + (128 * wave + 2 * lane) * 4, hra = lds0 + L_T2 + (128 * wave + (lane & 1) * 64 + 4 * rg) * 4, qwa = L_QP + (wave * 48 + c16 * 24 + rg * 8) * 4;
    unsigned qra = lds0 + L_QP + (lb * 24 + pos) * 4, sta = lds0 + L_ST + lb * 8, o4o = W_O4 + (unsigned)jb * 10240 + (unsigned)tid * 16;
    float b1f = bf1(bsm);
    PIN(hwa); PIN(hra); PIN(qwa); PIN(qra); PIN(sta); PIN(o4o); PIN(b1f);
    {
      unsigned off[1] = {W_H3 + (unsigned)(jb % H3R) * H3S + (unsigned)((lane >> 5) * 160 + 32 * wave + (lane & 31)) * 16};
      u32x4 v[1];
      get_units<1>(rs, off, v, true, tA, tB);
      STAMP(7);
      DSW64(hwa, (u32x2{v[0][1], v[0][3]}), 0);
      u32x4 hb[4];
#pragma unroll
      for (int t = 0; t < 4; ++t) DSR128(hb[t], hra, 64 * t);
      f32x4 d0 = {0.f, 0.f, 0.f, 0.f}, d1 = {0.f, 0.f, 0.f, 0.f};
      asm volatile("s_waitcnt lgkmcnt(2)" : "+v"(hb[0]), "+v"(hb[1])::"memory");
#pragma unroll
      for (int t = 0; t < 2; ++t) { d0 = mfma16(w1p[2 * t], hb[t], d0); d1 = mfma16(w1p[2 * t + 1], hb[t], d1); }
      asm volatile("s_waitcnt lgkmcnt(0)" : "+v"(hb[2]), "+v"(hb[3])::"memory");
#pragma unroll
      for (int t = 2; t < 4; ++t) { d0 = mfma16(w1p[2 * t], hb[t], d0); d1 = mfma16(w1p[2 * t + 1], hb[t], d1); }
      if (c16 < 2 && rg < 3) { f32x4* p = reinterpret_cast<f32x4*>(lds + qwa); p[0] = d0; p[1] = d1; }
    }
    lds_barrier();
    {
      float sp[5];
      f32x2 mr;
#pragma unroll
      for (int k = 0; k < 5; ++k) DSR32(sp[k], qra, k * 192);
      DSR64(mr, sta, 0);
      asm volatile("s_waitcnt lgkmcnt(0)" : "+v"(sp[0]), "+v"(sp[1]), "+v"(sp[2]), "+v"(sp[3]), "+v"(sp[4]), "+v"(mr)::"memory");
      const float s = ((sp[0] + sp[1]) + (sp[2] + sp[3])) + sp[4];
      const float hv = rbf(rbf(fmaf(-mr[0], csum, s) * mr[1]) + b1f);
      const float rr = hv > 0.f ? hv : 0.f;
      const float r2 = rr * rr;
      const unsigned rp2 = pack2(r2, dpp<XOR1>(r2));
      float a00 = 0.f, a01 = 0.f, a10 = 0.f, a11 = 0.f;
#pragma unroll
      for (int i = 0; i < 12; ++i) {
        const unsigned p0 = (unsigned)__builtin_amdgcn_readlane((int)rp2, 2 * i), p1 = (unsigned)__builtin_amdgcn_readlane((int)rp2, 24 + 2 * i);
        a00 = dot2(w2v[2 * i], p0, a00); a01 = dot2(w2v[2 * i + 1], p0, a01); a10 = dot2(w2v[2 * i], p1, a10); a11 = dot2(w2v[2 * i + 1], p1, a11);
      }
      {
        put_unit(rs, o4o, __float_as_uint(a00), __float_as_uint(a01), tA, tB);
        put_unit(rs, o4o + 5120, __float_as_uint(a10), __float_as_uint(a11), tA, tB);
      }
    }
  }
  STAMP(8);
}

extern "C" void kernel_launch(void* const* d_in, const int* in_sizes, int n_in, void* d_out, int out_size, void* d_ws, size_t ws_size, hipStream_t stream) {
  fused_kernel<<<256, NTHR, 0, stream>>>((const u16*)d_in[0], (const u16*)d_in[1], (const u16*)d_in[2], (const u16*)d_in[3], (const u16*)d_in[4], (const u16*)d_in[6],
                                         (const u16*)d_in[7], (const u16*)d_in[8], (const u16*)d_in[9], (u16*)d_out, (unsigned char*)d_ws);
}
```
